```python
import torch

def _softmax(x, axis=-1):
    return torch.softmax(x, dim=axis)

def _gelu(x):
    return torch.nn.functional.gelu(x, approximate='tanh')

def _silu(x):
    return torch.nn.functional.silu(x)

def _sigmoid(x):
    return torch.sigmoid(x)

def _layernorm(x, axis=-1, eps=1e-05):
    m = x.mean(dim=axis, keepdim=True)
    v = x.var(dim=axis, keepdim=True, unbiased=False)
    return (x - m) / torch.sqrt(v + eps)

def _scatter_add(tgt, idx, src, axis=0):
    return tgt.clone().index_add_(axis, idx, src)

def _segment_sum(src, seg, n):
    out = torch.zeros((n,) + src.shape[1:], dtype=src.dtype)
    return out.index_add_(0, seg, src)

def _pad(x, pw):
    flat = tuple((v for pair in reversed(pw) for v in pair))
    return torch.nn.functional.pad(x, flat)
kernel_name = 'adaLN_cond__gqa_block_cached__glu_gated_geglu__decode'

def setup_inputs():

    _w = lambda a: (lambda t: t.to(torch.bfloat16) if t.is_floating_point() else t)(torch.as_tensor(a))

    def _A_setup():
        B, Sq, D = (2, 1, 512)
        Hq, Hkv, Sk, Dh = (32, 8, 32768, 128)
        assert Hq % Hkv == 0, (Hq, Hkv)
        return {'x': _w((lambda *s: torch.randn(*s, dtype=torch.bfloat16) * 0.05)(B, Sq, D)), 'k_cache': _w((lambda *s: torch.randn(*s, dtype=torch.bfloat16) * 0.8)(B, Hkv, Sk, Dh)), 'v_cache': _w((lambda *s: torch.randn(*s, dtype=torch.bfloat16) * 3.2)(B, Hkv, Sk, Dh)), 'wq': _w((lambda *s: torch.randn(*s, dtype=torch.bfloat16) * 0.8)(D, Hq * Dh) * D ** (-0.5)), 'wo': _w((lambda *s: torch.randn(*s, dtype=torch.bfloat16) * 3.2)(Hq * Dh, D) * (Hq * Dh) ** (-0.5))}
    _A_in = _A_setup()

    def _B_setup():
        B, S, Dm, Df = (2, 1, 512, 1792)
        return {'x': _w((lambda *s: torch.randn(*s, dtype=torch.bfloat16) * 0.05)(B, S, Dm)), 'w_gate': _w((lambda *s: torch.randn(*s, dtype=torch.bfloat16) * 3.2)(Dm, Df) * Dm ** (-0.5)), 'w_up': _w((lambda *s: torch.randn(*s, dtype=torch.bfloat16) * 3.2)(Dm, Df) * Dm ** (-0.5)), 'w_down': _w((lambda *s: torch.randn(*s, dtype=torch.bfloat16) * 3.2)(Df, Dm) * Df ** (-0.5))}
    _B_in = _B_setup()
    return {'A_x': _A_in['x'], 'A_k_cache': _A_in['k_cache'], 'A_v_cache': _A_in['v_cache'], 'A_wq': _A_in['wq'], 'A_wo': _A_in['wo'], 'B_x': _B_in['x'], 'B_w_gate': _B_in['w_gate'], 'B_w_up': _B_in['w_up'], 'B_w_down': _B_in['w_down'], 'c': _w((lambda *s: torch.randn(*s, dtype=torch.bfloat16) * 0.4)(2, 512) * 8.0), 'w_mod': _w((lambda *s: torch.randn(*s, dtype=torch.bfloat16) * 0.4)(512, 6 * 512) * 512 ** (-0.5) * 8.0)}

def reference(A_x, A_k_cache, A_v_cache, A_wq, A_wo, B_x, B_w_gate, B_w_up, B_w_down, c, w_mod):

    def _A_ref(x, k_cache, v_cache, wq, wo):
        B, Sq, D = x.shape
        Hq, Dh = (32, 128)
        Hkv = k_cache.shape[1]
        rep = Hq // Hkv
        q = (lambda x, *d: x.permute(*d))((x @ wq).reshape(B, Sq, Hq, Dh), 0, 2, 1, 3)
        k = (lambda x, n, axis: torch.repeat_interleave(x, n, dim=axis))(k_cache, rep, axis=1)
        v = (lambda x, n, axis: torch.repeat_interleave(x, n, dim=axis))(v_cache, rep, axis=1)
        scores = torch.einsum('bhqd,bhkd->bhqk', q, k) * Dh ** (-0.5)
        p = _softmax(scores, axis=-1)
        o = torch.einsum('bhqk,bhkd->bhqd', p, v)
        return (lambda x, *d: x.permute(*d))(o, 0, 2, 1, 3).reshape(B, Sq, Hq * Dh) @ wo

    def _B_ref(x, w_gate, w_up, w_down):
        g = torch.einsum('bsd,df->bsf', x, w_gate)
        u = torch.einsum('bsd,df->bsf', x, w_up)
        h = _gelu(g) * u
        return torch.einsum('bsf,fd->bsd', h, w_down)
    x = A_x
    mod = (_silu(c) @ w_mod).reshape(c.shape[0], 6, 1, -1)
    sa, ca, ga, sb, cb, gb = (mod[:, 0].clone(), mod[:, 1].clone(), mod[:, 2].clone(), mod[:, 3].clone(), mod[:, 4].clone(), mod[:, 5].clone())
    h = _layernorm(x, axis=-1) * (1 + ca) + sa
    x = x + ga * _A_ref(h, A_k_cache, A_v_cache, A_wq, A_wo)
    h = _layernorm(x, axis=-1) * (1 + cb) + sb
    x = x + gb * _B_ref(h, B_w_gate, B_w_up, B_w_down)
    return x
```

```cpp
#include <hip/hip_runtime.h>
#include <hip/hip_bf16.h>
#include <stdint.h>

#define PACE 3
#define N1 13
#define N2 28
#define TW 0
#define WAUX 2
#define RND_S 1

using u32x2 = __attribute__((ext_vector_type(2))) unsigned;
using u32x4 = __attribute__((ext_vector_type(4))) unsigned;
using f32x4 = __attribute__((ext_vector_type(4))) float;
using bf16x2 = __attribute__((ext_vector_type(2))) __bf16;
using bf16x8 = __attribute__((ext_vector_type(8))) __bf16;
using f32x2 = __attribute__((ext_vector_type(2))) float;
typedef __amdgpu_buffer_rsrc_t rsrc_t;
typedef const __attribute__((address_space(1))) u32x4 gx4;

constexpr int SC1 = 16, NT = 2;
constexpr int WS_EPOCH = 0;
constexpr int WS_H = 4096;
constexpr int WS_CLR = 16384;
constexpr int WS_Q = WS_CLR;
constexpr int WS_Y = WS_CLR + 16384;
constexpr int WS_MOD = WS_CLR + 49152;
constexpr int WS_F = WS_CLR + 57344;
constexpr int WS_O = 131072;
constexpr int O_STRIDE = 1152;
constexpr int WS_BYTES = WS_O + 256 * O_STRIDE;
constexpr unsigned EMPTY = 0xFFFFFFFFu;
constexpr unsigned SPIN_CAP = 1u << 18;
constexpr int OOB = 0x7FFFFFF0;

struct Args {
  const void *x, *k, *v, *wq, *wo, *wg, *wu, *wd, *c, *wm;
  void *out, *ws;
};

__device__ __forceinline__ rsrc_t mk(const void* p, unsigned bytes) { return __builtin_amdgcn_make_buffer_rsrc((void*)p, 0, bytes, 0x00020000); }
__device__ __forceinline__ float asf(unsigned u) { return __builtin_bit_cast(float, u); }
__device__ __forceinline__ unsigned asu(float f) { return __builtin_bit_cast(unsigned, f); }
__device__ __forceinline__ float blo(unsigned u) { return asf(u << 16); }
__device__ __forceinline__ float bhi(unsigned u) { return asf(u & 0xffff0000u); }
__device__ __forceinline__ unsigned pk(float lo, float hi) {
  f32x2 v = {lo, hi};
  return __builtin_bit_cast(unsigned, __builtin_convertvector(v, bf16x2));
}
__device__ __forceinline__ float rb(float x) { return (float)(__bf16)x; }
__device__ __forceinline__ unsigned live(unsigned u) { return u == EMPTY ? 0x7FFF7FFFu : u; }
__device__ __forceinline__ float dot2(unsigned a, unsigned b, float c) {
  return __builtin_amdgcn_fdot2_f32_bf16(__builtin_bit_cast(bf16x2, a), __builtin_bit_cast(bf16x2, b), c, false);
}
template <int CTRL> __device__ __forceinline__ float dpp(float x) { return asf(__builtin_amdgcn_mov_dpp(asu(x), CTRL, 0xf, 0xf, true)); }
template <int CTRL> __device__ __forceinline__ unsigned dppu(unsigned x) { return __builtin_amdgcn_mov_dpp(x, CTRL, 0xf, 0xf, true); }
template <int CTRL, int BANK> __device__ __forceinline__ float dppb(float old, float x) {
  return asf(__builtin_amdgcn_update_dpp(asu(old), asu(x), CTRL, 0xf, BANK, false));
}
constexpr int QID = 0xE4, XOR1 = 0xB1, XOR2 = 0x4E, SHL4 = 0x104, SHL8 = 0x108, SHL12 = 0x10C, SHR4 = 0x114, ROR4 = 0x124, ROR8 = 0x128, ROR12 = 0x12C;
__device__ __forceinline__ float sw32(float a, float b) {
  auto s = __builtin_amdgcn_permlane32_swap(asu(a), asu(b), false, false);
  return asf(s[0]) + asf(s[1]);
}
__device__ __forceinline__ float sw16(float a, float b) {
  auto s = __builtin_amdgcn_permlane16_swap(asu(a), asu(b), false, false);
  return asf(s[0]) + asf(s[1]);
}
__device__ __forceinline__ float rows_max(float x) {
  auto s = __builtin_amdgcn_permlane16_swap(asu(x), asu(x), false, false);
  x = fmaxf(asf(s[0]), asf(s[1]));
  auto t = __builtin_amdgcn_permlane32_swap(asu(x), asu(x), false, false);
  return fmaxf(asf(t[0]), asf(t[1]));
}
__device__ __forceinline__ float wave_sum(float x) {
  x += dpp<XOR1>(x); x += dpp<XOR2>(x); x += dpp<ROR4>(x); x += dpp<ROR8>(x);
  x = sw16(x, x);
  return sw32(x, x);
}
__device__ __forceinline__ void bar() { asm volatile("s_waitcnt lgkmcnt(0)\n\ts_barrier" ::: "memory"); }
#define FENCE() __builtin_amdgcn_sched_barrier(0)
__device__ __forceinline__ void pair_rows(const u32x4& r0, const u32x4& r1, unsigned (&w)[8]) {
#pragma unroll
  for (int e = 0; e < 4; ++e) { w[2 * e] = __builtin_amdgcn_perm(r1[e], r0[e], 0x05040100); w[2 * e + 1] = __builtin_amdgcn_perm(r1[e], r0[e], 0x07060302); }
}
__device__ __forceinline__ float red8(const float (&s1)[8], int lane) {
  float s2[4];
#pragma unroll
  for (int j = 0; j < 4; ++j) s2[j] = sw16(s1[j], s1[j + 4]);
  float s3[2];
#pragma unroll
  for (int j = 0; j < 2; ++j) { const float t = s2[j] + dpp<ROR8>(s2[j]), u = s2[j + 2] + dpp<ROR8>(s2[j + 2]); s3[j] = (lane & 8) ? u : t; }
  const float t = s3[0] + dpp<SHL4>(s3[0]), u = s3[1] + dpp<SHR4>(s3[1]);
  return (lane & 4) ? u : t;
}
__device__ __forceinline__ float gemv16(const unsigned (&w)[8], unsigned a0, unsigned a1, int lane) {
  float s1[8];
#pragma unroll
  for (int j = 0; j < 8; ++j) s1[j] = sw32(dot2(w[j], a0, 0.f), dot2(w[j], a1, 0.f));
  const float r = red8(s1, lane);
  return r + dpp<XOR2>(r);
}
__device__ __forceinline__ bool any_empty(const u32x4& v) { return __builtin_amdgcn_ballot_w64(v[0] == EMPTY || v[1] == EMPTY || v[2] == EMPTY || v[3] == EMPTY) != 0; }
__device__ __forceinline__ float sum8(const float* red, int lane) {
  const float* p = red + (lane >> 5) * 128 + (lane & 31);
  const float v = (p[0] + p[32]) + (p[64] + p[96]);
  return sw32(v, v);
}

#define ST(n)

__global__ void __launch_bounds__(512, 2) fused_block(Args a) {
  __shared__ __attribute__((aligned(16))) float lds[4096 + 5 * 256 + 64 + 512 + 16];
  float* const l_o = lds;
  float* const l_red = lds + 4096;
  float* const l_mx = l_red + 5 * 256;
  float* const l_ls = l_mx + 32;
  unsigned* const l_x1 = (unsigned*)(l_ls + 32);
  float* const l_st = (float*)(l_x1 + 512);
  const int tid = threadIdx.x, lane = tid & 63, wave = __builtin_amdgcn_readfirstlane(tid >> 6);
  const int bid = blockIdx.x, g = bid & 7, i = bid >> 3, b = i >> 4, split = i & 15;
  ST(0);
  const rsrc_t rws = mk(a.ws, WS_BYTES);
  const size_t kvbase = ((size_t)(b * 8 + g) * 32768 + split * 2048) * 256;
  const rsrc_t rk = mk((const char*)a.k + kvbase, 524288), rv = mk((const char*)a.v + kvbase, 524288);
  const int slot = (lane >> 5) * 16 + (lane & 1) * 8 + ((lane >> 2) & 7);
  const int half = lane & 1, k0 = wave * 64 + (lane >> 1) * 2;

  const u32x2 ep = __builtin_amdgcn_raw_buffer_load_b64(rws, WS_EPOCH, 0, SC1);
  {
    const u32x4 e4 = {EMPTY, EMPTY, EMPTY, EMPTY};
    int off = OOB;
    if (wave == 0) off = WS_O + (g * 32 + i) * O_STRIDE + lane * 16;
    if (wave == 1 && lane < 2) off = WS_O + (g * 32 + i) * O_STRIDE + 1024 + lane * 16;
    if (wave == 2 && i < 8) off = WS_CLR + (g * 8 + i) * 1024 + lane * 16;
    __builtin_amdgcn_raw_buffer_store_b128(e4, rws, off, 0, SC1);
  }
  const rsrc_t rc = mk(a.c, 2048), rx = mk(a.x, 2048);
  const unsigned c0 = __builtin_amdgcn_raw_buffer_load_b32(rc, k0 * 2, 0, 0), c1 = __builtin_amdgcn_raw_buffer_load_b32(rc, k0 * 2 + 1024, 0, 0);
  const int pt = i >> 3;
  const int sec = pt == 0 ? half : pt == 1 ? 2 + 3 * half : 3 + half;
  const int mcol = g * 64 + (i & 7) * 8;
  const rsrc_t rm = mk(a.wm, 512 * 3072 * 2);
  const int moff = pt < 3 ? (k0 * 3072 + sec * 512 + mcol) * 2 : OOB;
  const u32x4 wm0 = __builtin_amdgcn_raw_buffer_load_b128(rm, moff, 0, WAUX), wm1 = __builtin_amdgcn_raw_buffer_load_b128(rm, moff, 6144, WAUX);
  const u32x4 xr0 = __builtin_amdgcn_raw_buffer_load_b128(rx, lane * 16, 0, 0), xr1 = __builtin_amdgcn_raw_buffer_load_b128(rx, lane * 16 + 1024, 0, 0);
  const unsigned short xs = __builtin_amdgcn_raw_buffer_load_b16(rx, (((lane >> 4) & 1) * 512 + mcol + (lane & 7)) * 2, 0, 0);
  const rsrc_t rq = mk(a.wq, 512 * 4096 * 2);
  const int qoff = (k0 * 4096 + g * 512 + i * 16 + half * 8) * 2;
  const u32x4 wq0 = __builtin_amdgcn_raw_buffer_load_b128(rq, qoff, 0, WAUX), wq1 = __builtin_amdgcn_raw_buffer_load_b128(rq, qoff, 8192, WAUX);

  u32x4 wo0, wo1, wf0, wf1, wd0, wd1, wd2, wd3;
  const int fblk = g * 28 + i;
  const int dcol = g * 64 + i * 8;
  auto load_tail = [&]() {
    const rsrc_t ro = mk(a.wo, 4096 * 512 * 2);
    const int ko = g * 512 + wave * 64 + ((lane >> 2) & 1) * 32 + ((lane >> 3) & 3) * 8 + (lane & 3) * 2;
    const int ooff = (ko * 512 + i * 16 + (lane >> 5) * 8) * 2;
    wo0 = __builtin_amdgcn_raw_buffer_load_b128(ro, ooff, 0, WAUX); wo1 = __builtin_amdgcn_raw_buffer_load_b128(ro, ooff, 1024, WAUX);
    const char* pf = (const char*)(half ? a.wu : a.wg) + (i < 28 ? (k0 * 1792 + fblk * 8) * 2 : 0);
    if (WAUX == NT) { wf0 = __builtin_nontemporal_load((gx4*)pf); wf1 = __builtin_nontemporal_load((gx4*)(pf + 3584)); }
    else { wf0 = *(gx4*)pf; wf1 = *(gx4*)(pf + 3584); }
    const rsrc_t rd = mk(a.wd, 1792 * 512 * 2);
    const int doff = (i < 8 && tid < 448) ? (tid * 4 * 512 + dcol) * 2 : OOB;
    wd0 = __builtin_amdgcn_raw_buffer_load_b128(rd, doff, 0, WAUX); wd1 = __builtin_amdgcn_raw_buffer_load_b128(rd, doff, 1024, WAUX);
    wd2 = __builtin_amdgcn_raw_buffer_load_b128(rd, doff, 2048, WAUX); wd3 = __builtin_amdgcn_raw_buffer_load_b128(rd, doff, 3072, WAUX);
  };

  u32x4 win[4][8];
  const int koff = ((lane >> 2) & 3) * 256 + ((lane >> 4) * 4 + (lane & 3)) * 16, voff = lane * 16;
  auto issue1 = [&](int blk, int n) {
    const int so = ((((blk & 7) + i) & 7) * 16 + (n >> 2) * 8 + wave) * 4096;
    if (blk < 8) win[blk & 3][n] = __builtin_amdgcn_raw_buffer_load_b128(rk, koff + (n & 3) * 1024, so, NT);
    else win[blk & 3][n] = __builtin_amdgcn_raw_buffer_load_b128(rv, voff + (n & 3) * 1024, so, NT);
  };
#define ISSUE(from, to) _Pragma("unroll") for (int p_ = (from); p_ < (to); ++p_) issue1(p_ >> 3, p_ & 7)
#define PACED(from, to) _Pragma("unroll") for (int p_ = (from); p_ < (to); ++p_) { \
    FENCE(); asm volatile("s_waitcnt vmcnt(%0)" :: "i"(PACE - 1) : "memory"); issue1(p_ >> 3, p_ & 7); FENCE(); }
  FENCE();
  if (TW == 0) load_tail();
  else { ISSUE(0, PACE); }
  FENCE();

  const unsigned long long tag = ((((unsigned long long)__builtin_amdgcn_readfirstlane(ep[1]) << 32) | (unsigned)__builtin_amdgcn_readfirstlane(ep[0])) + 1);
  const unsigned tag_lo = (unsigned)tag << 16, tag_hi = (unsigned)(tag >> 16);
  {
    auto silu = [](float v) { return v / (1.f + __expf(-v)); };
    unsigned w[8]; pair_rows(wm0, wm1, w);
    const float r = gemv16(w, pk(silu(blo(c0)), silu(bhi(c0))), pk(silu(blo(c1)), silu(bhi(c1))), lane);
    if (!(lane & 2)) l_red[wave * 32 + slot] = r;
  }
  if (wave < 3) asm volatile("s_waitcnt vmcnt(0)" ::: "memory");
  bar();
  unsigned modv = 0;
  if (wave == 0) {
    const float mv = rb(sum8(l_red, lane));
    modv = live(pk(mv, dpp<XOR1>(mv)));
    if (pt == 0) {
      float s0 = 0.f, s1 = 0.f;
#pragma unroll
      for (int e = 0; e < 4; ++e) { s0 += blo(xr0[e]) + bhi(xr0[e]); s1 += blo(xr1[e]) + bhi(xr1[e]); }
      const float m0 = wave_sum(s0) * (1.f / 512), m1 = wave_sum(s1) * (1.f / 512);
      float q0 = 0.f, q1 = 0.f;
#pragma unroll
      for (int e = 0; e < 4; ++e) {
        float d = blo(xr0[e]) - m0; q0 += d * d; d = bhi(xr0[e]) - m0; q0 += d * d;
        d = blo(xr1[e]) - m1; q1 += d * d; d = bhi(xr1[e]) - m1; q1 += d * d;
      }
      const float v0 = wave_sum(q0) * (1.f / 512), v1 = wave_sum(q1) * (1.f / 512);
      const float mean = (lane & 16) ? m1 : m0, var = (lane & 16) ? v1 : v0;
      const float ln = rb(rb(blo(xs) - rb(mean)) / rb(sqrtf(rb(rb(var) + 1e-5f))));
      const float ca = dpp<ROR8>(mv);
      const float h = rb(rb(ln * rb(1.f + ca)) + mv);
      const int col = mcol + (lane & 7);
      const int off = (lane < 32 && !(lane & 8)) ? WS_H + ((col >> 1) * 4 + ((lane >> 4) & 1) * 2 + (col & 1)) * 8 : OOB;
      const u32x2 gr = {tag_lo | (asu(h) >> 16), tag_hi};
      __builtin_amdgcn_raw_buffer_store_b64(gr, rws, off, 0, SC1);
    }
  }
  ST(1);
  if (TW == 0) { FENCE(); ISSUE(0, PACE); }
  PACED(PACE, N1);
  {
    const int hoff = WS_H + (k0 >> 1) * 32 + half * 16;
    u32x2 h0 = __builtin_amdgcn_raw_buffer_load_b64(rws, hoff, 0, SC1), h1 = __builtin_amdgcn_raw_buffer_load_b64(rws, hoff + 8, 0, SC1);
    FENCE();
    ISSUE(N1, N1 + PACE);
    FENCE();
    unsigned w[8]; pair_rows(wq0, wq1, w);
    auto stale = [&]() { return __builtin_amdgcn_ballot_w64((h0[1] != tag_hi) | (h1[1] != tag_hi) | ((h0[0] & 0xffff0000u) != tag_lo) | ((h1[0] & 0xffff0000u) != tag_lo)) != 0; };
    if (stale()) {
      unsigned sp = 0;
#pragma nounroll
      do {
        __builtin_amdgcn_s_sleep(2);
        asm volatile("" ::: "memory");
        h0 = __builtin_amdgcn_raw_buffer_load_b64(rws, hoff, 0, SC1); h1 = __builtin_amdgcn_raw_buffer_load_b64(rws, hoff + 8, 0, SC1);
      } while (stale() && ++sp < SPIN_CAP);
    }
    ST(2);
    const unsigned hp = __builtin_amdgcn_perm(h1[0], h0[0], 0x05040100), ho = dppu<XOR1>(hp);
    const float r = gemv16(w, half ? ho : hp, half ? hp : ho, lane);
    if (!(lane & 2)) l_red[256 + wave * 32 + slot] = r;
  }
  bar();
  if (wave == 0) {
    const float qv = sum8(l_red + 256, lane);
    const unsigned qp = live(pk(qv, dpp<XOR1>(qv)));
    const int bb = (lane >> 4) & 1;
    int off = OOB;
    if (!(lane & 1)) {
      if (lane < 32) off = WS_Q + ((g * 2 + bb) * 512 + i * 16 + (lane & 15)) * 2;
      else if (pt == 1 || pt == 2) off = WS_MOD + ((((pt - 1) * 2 + ((lane >> 3) & 1)) * 2 + bb) * 512 + mcol + (lane & 7)) * 2;
    }
    __builtin_amdgcn_raw_buffer_store_b32(lane < 32 ? qp : modv, rws, off, 0, SC1);
  }
  ST(3);
  PACED(N1 + PACE, N2);
  u32x4 qf;
  {
    const int off = WS_Q + ((g * 2 + b) * 512 + (lane & 3) * 128 + ((lane >> 4) * 4 + ((lane >> 2) & 3)) * 8) * 2;
    qf = __builtin_amdgcn_raw_buffer_load_b128(rws, off, 0, SC1);
    FENCE();
    ISSUE(N2, 32);
    FENCE();
    if (any_empty(qf)) {
      unsigned sp = 0;
#pragma nounroll
      do {
        __builtin_amdgcn_s_sleep(2);
        asm volatile("" ::: "memory");
        qf = __builtin_amdgcn_raw_buffer_load_b128(rws, off, 0, SC1);
      } while (any_empty(qf) && ++sp < SPIN_CAP);
    }
  }
  ST(4);

  float S[16];
#pragma unroll
  for (int blk = 0; blk < 8; ++blk) {
#pragma unroll
    for (int t = 0; t < 2; ++t) {
      f32x4 d[4];
#pragma unroll
      for (int x = 0; x < 4; ++x)
        d[x] = __builtin_amdgcn_mfma_f32_16x16x32_bf16(__builtin_bit_cast(bf16x8, win[blk & 3][t * 4 + x]), __builtin_bit_cast(bf16x8, qf), f32x4{0.f, 0.f, 0.f, 0.f}, 0, 0, 0);
      const float t0 = dppb<QID, 8>(dppb<QID, 4>(dppb<QID, 2>(d[0][0], d[1][1]), d[2][2]), d[3][3]);
      const float t1 = dppb<ROR4, 8>(dppb<ROR4, 4>(dppb<ROR4, 2>(dpp<ROR4>(d[0][3]), d[1][0]), d[2][1]), d[3][2]);
      const float t2 = dppb<ROR8, 8>(dppb<ROR8, 4>(dppb<ROR8, 2>(dpp<ROR8>(d[0][2]), d[1][3]), d[2][0]), d[3][1]);
      const float t3 = dppb<ROR12, 8>(dppb<ROR12, 4>(dppb<ROR12, 2>(dpp<ROR12>(d[0][1]), d[1][2]), d[2][3]), d[3][0]);
      const float s = (t0 + t1) + (t2 + t3);
      S[blk * 2 + t] = RND_S ? rb(rb(s) * 0.08838834764831845f) : s * 0.08838834764831845f;
    }
    FENCE();
    ISSUE((blk + 4) * 8, (blk + 5) * 8);
    FENCE();
  }
  float mx = S[0];
#pragma unroll
  for (int u = 1; u < 16; ++u) mx = fmaxf(mx, S[u]);
  mx = fmaxf(mx, dpp<ROR4>(mx)); mx = fmaxf(mx, dpp<ROR8>(mx)); mx = rows_max(mx);
  if (lane < 4) l_mx[lane * 8 + wave] = mx;
  bar();
  ST(5);
  {
    const f32x4 m0 = *(const f32x4*)(l_mx + (lane & 3) * 8), m1 = *(const f32x4*)(l_mx + (lane & 3) * 8 + 4);
    mx = fmaxf(fmaxf(fmaxf(m0[0], m0[1]), fmaxf(m0[2], m0[3])), fmaxf(fmaxf(m1[0], m1[1]), fmaxf(m1[2], m1[3])));
  }
  constexpr float LOG2E = 1.4426950408889634f;
  unsigned P[8];
  {
    float ls = 0.f;
#pragma unroll
    for (int u = 0; u < 8; ++u) {
      const float p0 = __builtin_amdgcn_exp2f((S[2 * u] - mx) * LOG2E), p1 = __builtin_amdgcn_exp2f((S[2 * u + 1] - mx) * LOG2E);
      ls += p0 + p1;
      P[u] = pk(p0, p1);
    }
    ls += dpp<ROR4>(ls); ls += dpp<ROR8>(ls); ls = sw16(ls, ls); ls = sw32(ls, ls);
    if (lane < 4) l_ls[lane * 8 + wave] = ls;
  }
  f32x4 acc[8];
#pragma unroll
  for (int j = 0; j < 8; ++j) acc[j] = f32x4{0.f, 0.f, 0.f, 0.f};
  unsigned xg0, xg1, ms0, ms1, mc0, mc1;
  const int ycol = (wave & 3) * 128 + (lane & 31) * 4 + (lane >> 5) * 2, yb = wave >> 2;
  auto load_mod = [&]() {
    xg1 = __builtin_amdgcn_raw_buffer_load_b32(rws, WS_MOD + (yb * 512 + ycol) * 2, 0, SC1);
    ms0 = __builtin_amdgcn_raw_buffer_load_b32(rws, WS_MOD + (4 * 512 + k0) * 2, 0, SC1); ms1 = __builtin_amdgcn_raw_buffer_load_b32(rws, WS_MOD + (5 * 512 + k0) * 2, 0, SC1);
    mc0 = __builtin_amdgcn_raw_buffer_load_b32(rws, WS_MOD + (6 * 512 + k0) * 2, 0, SC1); mc1 = __builtin_amdgcn_raw_buffer_load_b32(rws, WS_MOD + (7 * 512 + k0) * 2, 0, SC1);
  };
  unsigned gbp;
  auto load_gb = [&]() { gbp = __builtin_amdgcn_raw_buffer_load_b32(rws, WS_MOD + ((2 + ((lane >> 3) & 1)) * 512 + (dcol & 511) + (lane & 6)) * 2, 0, SC1); };
#pragma unroll
  for (int blk = 8; blk < 16; ++blk) {
    const unsigned p = P[blk - 8];
    const u32x4 pb = {p, dppu<SHL4>(p), dppu<SHL8>(p), dppu<SHL12>(p)};
#pragma unroll
    for (int j = 0; j < 8; ++j) {
      u32x4 va;
#pragma unroll
      for (int x = 0; x < 4; ++x) va[x] = __builtin_amdgcn_perm(win[blk & 3][4 + x][j >> 1], win[blk & 3][x][j >> 1], (j & 1) ? 0x07060302 : 0x05040100);
      acc[j] = __builtin_amdgcn_mfma_f32_16x16x32_bf16(__builtin_bit_cast(bf16x8, va), __builtin_bit_cast(bf16x8, pb), acc[j], 0, 0, 0);
    }
    FENCE();
    if (blk < 12) { ISSUE((blk + 4) * 8, (blk + 5) * 8); }
    if (blk == 12) {
      xg0 = __builtin_amdgcn_raw_buffer_load_b32(rx, (yb * 512 + ycol) * 2, 0, 0);
      load_mod(); load_gb();
      if (TW == 1) load_tail();
    }
    FENCE();
  }
  ST(6);
  if ((lane & 12) == 0) {
#pragma unroll
    for (int j = 0; j < 8; ++j) *(f32x4*)(l_o + wave * 512 + (lane & 3) * 128 + (lane >> 4) * 32 + j * 4) = acc[j];
  }
  bar();
  if (wave < 4) {
    const float* p = l_o + (lane >> 4) * 1024 + wave * 128 + (lane & 15) * 8;
    const f32x4 e = *(const f32x4*)p + *(const f32x4*)(p + 512), o = *(const f32x4*)(p + 4) + *(const f32x4*)(p + 516);
    const f32x4 l0 = *(const f32x4*)(l_ls + wave * 8), l1 = *(const f32x4*)(l_ls + wave * 8 + 4);
    const float l = ((l0[0] + l0[1]) + (l0[2] + l0[3])) + ((l1[0] + l1[1]) + (l1[2] + l1[3]));
    const float inv = 1.f / l;
    const float ee = sw16(sw32(e[0], e[2]), sw32(e[1], e[3])), oo = sw16(sw32(o[0], o[2]), sw32(o[1], o[3]));
    const int base = WS_O + (g * 32 + i) * O_STRIDE;
    __builtin_amdgcn_raw_buffer_store_b32(live(pk(ee * inv, oo * inv)), rws, base + wave * 256 + (lane & 15) * 16 + (lane >> 4) * 4, 0, SC1);
    const u32x2 ml = {live(asu(mx)), live(asu(l))};
    __builtin_amdgcn_raw_buffer_store_b64(ml, rws, lane == wave ? base + 1024 + wave * 8 : OOB, 0, SC1);
  }
  ST(7);
  {
    unsigned w[8]; pair_rows(wo0, wo1, w);
    const int src = WS_O + (g * 32 + (lane >> 5) * 16 + ((lane >> 3) & 3)) * O_STRIDE;
    const int ooff = src + wave * 128 + (lane & 7) * 16, loff = src + 1024 + (wave >> 1) * 8;
    u32x4 ov[4]; u32x2 ml[4];
    bool need[4] = {true, true, true, true};
#pragma nounroll
    for (unsigned sp = 0; sp < SPIN_CAP; ++sp) {
      asm volatile("" ::: "memory");
#pragma unroll
      for (int u = 0; u < 4; ++u) if (need[u]) {
        ov[u] = __builtin_amdgcn_raw_buffer_load_b128(rws, ooff, u * 4 * O_STRIDE, SC1);
        ml[u] = __builtin_amdgcn_raw_buffer_load_b64(rws, loff, u * 4 * O_STRIDE, SC1);
      }
#pragma unroll
      for (int u = 0; u < 4; ++u) need[u] = __builtin_amdgcn_ballot_w64(ov[u][0] == EMPTY || ov[u][1] == EMPTY || ov[u][2] == EMPTY || ov[u][3] == EMPTY || ml[u][0] == EMPTY || ml[u][1] == EMPTY) != 0;
      if (!(need[0] | need[1] | need[2] | need[3])) break;
      __builtin_amdgcn_s_sleep(1);
    }
    ST(8);
    float M = fmaxf(fmaxf(asf(ml[0][0]), asf(ml[1][0])), fmaxf(asf(ml[2][0]), asf(ml[3][0])));
    M = fmaxf(M, dpp<ROR8>(M));
    { auto s = __builtin_amdgcn_permlane16_swap(asu(M), asu(M), false, false); M = fmaxf(asf(s[0]), asf(s[1])); }
    float wt[4], L = 0.f;
#pragma unroll
    for (int u = 0; u < 4; ++u) { wt[u] = __builtin_amdgcn_exp2f((asf(ml[u][0]) - M) * LOG2E) * asf(ml[u][1]); L += wt[u]; }
    L += dpp<ROR8>(L); L = sw16(L, L);
    float e[4], o[4];
#pragma unroll
    for (int r = 0; r < 4; ++r) {
      e[r] = wt[0] * blo(ov[0][r]); o[r] = wt[0] * bhi(ov[0][r]);
#pragma unroll
      for (int u = 1; u < 4; ++u) { e[r] = fmaf(wt[u], blo(ov[u][r]), e[r]); o[r] = fmaf(wt[u], bhi(ov[u][r]), o[r]); }
    }
    const float e0 = sw16(e[0], e[2]), e1 = sw16(e[1], e[3]), o0 = sw16(o[0], o[2]), o1 = sw16(o[1], o[3]);
    const float ea = e0 + dpp<ROR8>(e0), eb = e1 + dpp<ROR8>(e1), oa = o0 + dpp<ROR8>(o0), ob = o1 + dpp<ROR8>(o1);
    const float inv = 1.f / L;
    const unsigned op = pk(((lane & 8) ? eb : ea) * inv, ((lane & 8) ? ob : oa) * inv);
    auto s = __builtin_amdgcn_permlane32_swap(op, op, false, false);
    float s0[8], s1[8];
#pragma unroll
    for (int j = 0; j < 8; ++j) { s0[j] = dot2(w[j], s[0], 0.f); s1[j] = dot2(w[j], s[1], 0.f); }
    const float r0 = red8(s0, lane), r1 = red8(s1, lane);
    const float t0 = r0 + dpp<XOR2>(r0), t1 = r1 + dpp<XOR2>(r1);
    float r = (lane & 2) ? t1 : t0;
    r += dpp<XOR1>(r);
    if (!(lane & 1)) l_red[512 + wave * 32 + ((lane >> 1) & 1) * 16 + (lane >> 5) * 8 + ((lane >> 2) & 7)] = r;
  }
  bar();
  if (wave == 0) {
    const float y = sum8(l_red + 512, lane);
    __builtin_amdgcn_raw_buffer_store_b32(live(asu(y)), rws, lane < 32 ? WS_Y + ((g * 2 + (lane >> 4)) * 512 + i * 16 + (lane & 15)) * 4 : OOB, 0, SC1);
  }
  ST(9);
  if (i < 28) {
    unsigned w[8]; pair_rows(wf0, wf1, w);
#pragma nounroll
    for (unsigned sp = 0; sp < SPIN_CAP; ++sp) {
      if (__builtin_amdgcn_ballot_w64(xg1 == EMPTY || ms0 == EMPTY || ms1 == EMPTY || mc0 == EMPTY || mc1 == EMPTY || gbp == EMPTY) == 0) break;
      asm volatile("" ::: "memory");
      load_mod(); load_gb();
    }
    const int yoff = WS_Y + (((lane >> 5) * 8 + yb) * 512 + (wave & 3) * 128 + (lane & 31) * 4) * 4;
    u32x4 yv[4];
    bool need[4] = {true, true, true, true};
#pragma nounroll
    for (unsigned sp = 0; sp < SPIN_CAP; ++sp) {
      asm volatile("" ::: "memory");
#pragma unroll
      for (int u = 0; u < 4; ++u) if (need[u]) yv[u] = __builtin_amdgcn_raw_buffer_load_b128(rws, yoff, u * 4096, SC1);
#pragma unroll
      for (int u = 0; u < 4; ++u) need[u] = any_empty(yv[u]);
      if (!(need[0] | need[1] | need[2] | need[3])) break;
      __builtin_amdgcn_s_sleep(1);
    }
    ST(10);
    float y[4];
#pragma unroll
    for (int r = 0; r < 4; ++r) y[r] = (asf(yv[0][r]) + asf(yv[1][r])) + (asf(yv[2][r]) + asf(yv[3][r]));
    const float xa = rb(blo(xg0) + rb(blo(xg1) * rb(sw32(y[0], y[2])))), xb = rb(bhi(xg0) + rb(bhi(xg1) * rb(sw32(y[1], y[3]))));
    l_x1[yb * 256 + (ycol >> 1)] = pk(xa, xb);
    const float mw = wave_sum(xa + xb) * (1.f / 128);
    const float m2 = wave_sum((xa - mw) * (xa - mw) + (xb - mw) * (xb - mw));
    if (lane == 0) { l_st[wave * 2] = mw; l_st[wave * 2 + 1] = m2; }
    bar();
    unsigned hp[2];
#pragma unroll
    for (int bb = 0; bb < 2; ++bb) {
      const f32x4 s0 = *(const f32x4*)(l_st + bb * 8), s1 = *(const f32x4*)(l_st + bb * 8 + 4);
      const unsigned xp = l_x1[bb * 256 + (k0 >> 1)];
      const float mean = ((s0[0] + s0[2]) + (s1[0] + s1[2])) * 0.25f;
      const float d0 = s0[0] - mean, d1 = s0[2] - mean, d2 = s1[0] - mean, d3 = s1[2] - mean;
      const float var = (((s0[1] + s0[3]) + (s1[1] + s1[3])) + 128.f * ((d0 * d0 + d1 * d1) + (d2 * d2 + d3 * d3))) * (1.f / 512);
      const float sd = rb(sqrtf(rb(rb(var) + 1e-5f))), mb = rb(mean);
      const unsigned ms = bb ? ms1 : ms0, mc = bb ? mc1 : mc0;
      hp[bb] = pk(rb(rb(rb(blo(xp) - mb) / sd) * rb(1.f + blo(mc))) + blo(ms), rb(rb(rb(bhi(xp) - mb) / sd) * rb(1.f + bhi(mc))) + bhi(ms));
    }
    const float r = gemv16(w, hp[0], hp[1], lane);
    if (!(lane & 2)) l_red[768 + wave * 32 + slot] = r;
    bar();
    if (wave == 0) {
      const float gv = rb(sum8(l_red + 768, lane)), uv = dpp<ROR8>(gv);
      const float th = 1.f - 2.f / (1.f + __expf(2.f * 0.7978845608028654f * (gv + 0.044715f * gv * gv * gv)));
      const float f = rb(0.5f * gv * (1.f + th)) * uv;
      const unsigned fp = live(pk(f, dpp<XOR1>(f)));
      __builtin_amdgcn_raw_buffer_store_b32(fp, rws, (lane & 41) == 0 ? WS_F + ((fblk * 4 + ((lane >> 1) & 3)) * 2 + (lane >> 4)) * 4 : OOB, 0, SC1);
    }
    ST(11);
    if (i < 8) {
      unsigned wa[8], wb[8]; pair_rows(wd0, wd1, wa); pair_rows(wd2, wd3, wb);
      const int foff = tid < 448 ? WS_F + tid * 16 : OOB;
      u32x4 fv;
#pragma nounroll
    for (unsigned sp = 0; sp < SPIN_CAP; ++sp) {
        asm volatile("" ::: "memory");
        fv = __builtin_amdgcn_raw_buffer_load_b128(rws, foff, 0, SC1);
        if (!any_empty(fv)) break;
        __builtin_amdgcn_s_sleep(1);
      }
      ST(12);
      float s1[8];
#pragma unroll
      for (int j = 0; j < 8; ++j) s1[j] = sw32(dot2(wa[j], fv[0], dot2(wb[j], fv[2], 0.f)), dot2(wa[j], fv[1], dot2(wb[j], fv[3], 0.f)));
      float r = red8(s1, lane);
      r += dpp<XOR2>(r); r += dpp<XOR1>(r);
      if (!(lane & 3)) l_red[1024 + wave * 32 + (lane >> 2)] = r;
      bar();
      if (wave == 0) {
        const float* p = l_red + 1024 + (lane >> 4) * 64 + (lane & 15);
        float y2 = p[0] + p[32];
        y2 = sw16(y2, y2); y2 = rb(sw32(y2, y2));
        const int bb = (lane >> 3) & 1, col = dcol + (lane & 7);
        const unsigned xp = l_x1[bb * 256 + (col >> 1)];
        const float x1 = (lane & 1) ? bhi(xp) : blo(xp), gb = (lane & 1) ? bhi(gbp) : blo(gbp);
        const float ov = x1 + rb(gb * y2);
        const unsigned op = pk(ov, dpp<XOR1>(ov));
        if (lane < 16 && !(lane & 1)) *(unsigned*)((char*)a.out + (bb * 512 + col) * 2) = op;
        if (bid == 0 && lane == 0) { const u32x2 ne = {(unsigned)tag, (unsigned)(tag >> 32)}; __builtin_amdgcn_raw_buffer_store_b64(ne, rws, WS_EPOCH, 0, SC1); }
      }
      ST(13);
    }
  }
}

extern "C" void kernel_launch(void* const* d_in, const int* in_sizes, int n_in, void* d_out, int out_size, void* d_ws, size_t ws_size, hipStream_t stream) {
  Args a;
  a.x = d_in[0]; a.k = d_in[1]; a.v = d_in[2]; a.wq = d_in[3]; a.wo = d_in[4];
  a.wg = d_in[6]; a.wu = d_in[7]; a.wd = d_in[8]; a.c = d_in[9]; a.wm = d_in[10];
  a.out = d_out; a.ws = d_ws;
  fused_block<<<256, 512, 0, stream>>>(a);
}
```

```cpp
#include <hip/hip_runtime.h>
#include <hip/hip_bf16.h>
#include <stdint.h>

#define PACE 3
#define N1 4
#define N2 26
#define TW 1
#define WAUX 2
#define RND_S 1

using u32x2 = __attribute__((ext_vector_type(2))) unsigned;
using u32x4 = __attribute__((ext_vector_type(4))) unsigned;
using f32x4 = __attribute__((ext_vector_type(4))) float;
using bf16x2 = __attribute__((ext_vector_type(2))) __bf16;
using bf16x8 = __attribute__((ext_vector_type(8))) __bf16;
using f32x2 = __attribute__((ext_vector_type(2))) float;
typedef __amdgpu_buffer_rsrc_t rsrc_t;

constexpr int SC1 = 16, NT = 2;
constexpr int WS_EPOCH = 0;
constexpr int WS_H = 16384;
constexpr int WS_Q = 32768;
constexpr int WS_Y = 98304;
constexpr int WS_MOD = WS_Y + 32768;
constexpr int WS_Z = WS_MOD + 8192;
constexpr int WS_O = 262144;
constexpr int O_STRIDE = 1152;
constexpr int WS_BYTES = WS_O + 256 * O_STRIDE;
constexpr unsigned EMPTY = 0xFFFFFFFFu;
constexpr unsigned SPIN_CAP = 1u << 18;
constexpr int OOB = 0x7FFFFFF0;

struct Args {
  const void *x, *k, *v, *wq, *wo, *wg, *wu, *wd, *c, *wm;
  void *out, *ws;
};

__device__ __forceinline__ rsrc_t mk(const void* p, unsigned bytes) { return __builtin_amdgcn_make_buffer_rsrc((void*)p, 0, bytes, 0x00020000); }
__device__ __forceinline__ float asf(unsigned u) { return __builtin_bit_cast(float, u); }
__device__ __forceinline__ unsigned asu(float f) { return __builtin_bit_cast(unsigned, f); }
__device__ __forceinline__ float blo(unsigned u) { return asf(u << 16); }
__device__ __forceinline__ float bhi(unsigned u) { return asf(u & 0xffff0000u); }
__device__ __forceinline__ unsigned pk(float lo, float hi) {
  f32x2 v = {lo, hi};
  return __builtin_bit_cast(unsigned, __builtin_convertvector(v, bf16x2));
}
__device__ __forceinline__ float rb(float x) { return (float)(__bf16)x; }
__device__ __forceinline__ unsigned live(unsigned u) { return u == EMPTY ? 0x7FFF7FFFu : u; }
__device__ __forceinline__ float dot2(unsigned a, unsigned b, float c) {
  return __builtin_amdgcn_fdot2_f32_bf16(__builtin_bit_cast(bf16x2, a), __builtin_bit_cast(bf16x2, b), c, false);
}
template <int CTRL> __device__ __forceinline__ float dpp(float x) { return asf(__builtin_amdgcn_mov_dpp(asu(x), CTRL, 0xf, 0xf, true)); }
template <int CTRL> __device__ __forceinline__ unsigned dppu(unsigned x) { return __builtin_amdgcn_mov_dpp(x, CTRL, 0xf, 0xf, true); }
template <int CTRL, int BANK> __device__ __forceinline__ float dppb(float old, float x) {
  return asf(__builtin_amdgcn_update_dpp(asu(old), asu(x), CTRL, 0xf, BANK, false));
}
constexpr int QID = 0xE4, XOR1 = 0xB1, XOR2 = 0x4E, SHL4 = 0x104, SHL8 = 0x108, SHL12 = 0x10C, SHR4 = 0x114, ROR4 = 0x124, ROR8 = 0x128, ROR12 = 0x12C;
__device__ __forceinline__ float sw32(float a, float b) {
  auto s = __builtin_amdgcn_permlane32_swap(asu(a), asu(b), false, false);
  return asf(s[0]) + asf(s[1]);
}
__device__ __forceinline__ float sw16(float a, float b) {
  auto s = __builtin_amdgcn_permlane16_swap(asu(a), asu(b), false, false);
  return asf(s[0]) + asf(s[1]);
}
__device__ __forceinline__ float rows_max(float x) {
  auto s = __builtin_amdgcn_permlane16_swap(asu(x), asu(x), false, false);
  x = fmaxf(asf(s[0]), asf(s[1]));
  auto t = __builtin_amdgcn_permlane32_swap(asu(x), asu(x), false, false);
  return fmaxf(asf(t[0]), asf(t[1]));
}
__device__ __forceinline__ float wave_sum(float x) {
  x += dpp<XOR1>(x); x += dpp<XOR2>(x); x += dpp<ROR4>(x); x += dpp<ROR8>(x);
  x = sw16(x, x);
  return sw32(x, x);
}
__device__ __forceinline__ void bar() { asm volatile("s_waitcnt lgkmcnt(0)\n\ts_barrier" ::: "memory"); }
#define FENCE() __builtin_amdgcn_sched_barrier(0)
__device__ __forceinline__ void xpose(const u32x4 (&t)[8], u32x4 (&o)[8]) {
#pragma unroll
  for (int j = 0; j < 8; ++j)
#pragma unroll
    for (int e = 0; e < 4; ++e) o[j][e] = __builtin_amdgcn_perm(t[2 * e + 1][j >> 1], t[2 * e][j >> 1], (j & 1) ? 0x07060302 : 0x05040100);
}
__device__ __forceinline__ f32x4 tile_mv(const u32x4 (&w)[8], unsigned ap) {
  const u32x4 av = {dppu<0x00>(ap), dppu<0x55>(ap), dppu<0xAA>(ap), dppu<0xFF>(ap)};
  float u[8];
#pragma unroll
  for (int j = 0; j < 8; ++j) {
    const f32x4 d = __builtin_amdgcn_mfma_f32_16x16x32_bf16(__builtin_bit_cast(bf16x8, av), __builtin_bit_cast(bf16x8, w[j]), f32x4{0.f, 0.f, 0.f, 0.f}, 0, 0, 0);
    u[j] = asf(__builtin_amdgcn_update_dpp(asu(d[0]), asu(d[0]), ROR8, 0xC, 0xf, false));
  }
  return f32x4{sw32(u[0], u[4]), sw32(u[1], u[5]), sw32(u[2], u[6]), sw32(u[3], u[7])};
}
__device__ __forceinline__ void put_red(float* red, int wave, int lane, const f32x4& r) {
  if (!(lane & 8)) *(f32x4*)(red + wave * 128 + ((lane >> 4) & 1) * 64 + (lane & 7) * 8 + (lane >> 5) * 4) = r;
}
__device__ __forceinline__ f32x2 fin_red(const float* red, int lane) {
  const float* p = red + (lane >> 5) * 512 + (lane & 31) * 4;
  const f32x4 v = (*(const f32x4*)p + *(const f32x4*)(p + 128)) + (*(const f32x4*)(p + 256) + *(const f32x4*)(p + 384));
  return f32x2{sw32(v[0], v[2]), sw32(v[1], v[3])};
}
__device__ __forceinline__ float sum_sameb(float x) {
  x += dpp<XOR1>(x); x += dpp<XOR2>(x); x += dpp<ROR8>(x);
  x = sw16(x, x);
  return sw32(x, x);
}
__device__ __forceinline__ float rcp(float x) { return __builtin_amdgcn_rcpf(x); }
__device__ __forceinline__ float modulate(float x, float mean, float isd, float c, float s) { return rb(rb(rb(x - mean) * isd) * rb(1.f + c)) + s; }
__device__ __forceinline__ bool any_empty(const u32x4& v) { return __builtin_amdgcn_ballot_w64((v[0] == EMPTY) | (v[1] == EMPTY) | (v[2] == EMPTY) | (v[3] == EMPTY)) != 0; }

#define ST(n)

__global__ void __launch_bounds__(512, 2) fused_block(Args a) {
  __shared__ __attribute__((aligned(16))) float lds[4096 + 2048 + 64 + 512 + 32 + 256];
  float* const l_o = lds;
  float* const l_red = lds + 4096;
  float* const l_mx = l_red + 2048;
  float* const l_ls = l_mx + 32;
  unsigned* const l_x1 = (unsigned*)(l_ls + 32);
  float* const l_st = (float*)(l_x1 + 512);
  unsigned short* const l_q = (unsigned short*)(l_st + 32);
  const int tid = threadIdx.x, lane = tid & 63, wave = __builtin_amdgcn_readfirstlane(tid >> 6);
  const int bid = blockIdx.x, g = bid & 7, i = bid >> 3, b = i >> 4, split = i & 15;
  ST(0);
  const rsrc_t rws = mk(a.ws, WS_BYTES);
  const size_t kvbase = ((size_t)(b * 8 + g) * 32768 + split * 2048) * 256;
  const rsrc_t rk = mk((const char*)a.k + kvbase, 524288), rv = mk((const char*)a.v + kvbase, 524288);
  const int mi = (i - 22) * 8 + g;
  const bool is_q = i < 8, is_wo = i >= 8 && i < 16, is_mod = i >= 16 && i < 22, is_mlp = i >= 22 && mi < 28;
  const int trow = ((lane >> 3) & 1) * 32 + (lane >> 4) * 8;
  const int ab = (lane >> 2) & 1, kp = wave * 64 + trow + (lane & 3) * 2;
  const int fb = (lane >> 4) & 1, fc = (lane & 15) * 4 + (lane >> 5) * 2;

  const u32x2 ep = __builtin_amdgcn_raw_buffer_load_b64(rws, WS_EPOCH, 0, SC1);
  {
    const u32x4 e4 = {EMPTY, EMPTY, EMPTY, EMPTY};
    const int cbase = is_q ? WS_O + (g * 8 + i) * 4608 : is_wo ? WS_Z + (g * 8 + i - 8) * 1792 : WS_Y + ((i - 16) * 8 + g) * 2560;
    const int cn = is_q ? 288 : is_wo ? 112 : (i == 16 || i == 17) ? 160 : 0;
    __builtin_amdgcn_raw_buffer_store_b128(e4, rws, tid < cn ? cbase + tid * 16 : OOB, 0, SC1);
  }
  const rsrc_t rc = mk(a.c, 2048), rx = mk(a.x, 2048);
  const unsigned cpair = __builtin_amdgcn_raw_buffer_load_b32(rc, (ab * 512 + kp) * 2, 0, 0);
  const unsigned xpair = __builtin_amdgcn_raw_buffer_load_b32(rx, (ab * 512 + kp) * 2, 0, 0);
  const u32x4 xr0 = __builtin_amdgcn_raw_buffer_load_b128(rx, lane * 16, 0, 0), xr1 = __builtin_amdgcn_raw_buffer_load_b128(rx, lane * 16 + 1024, 0, 0);
  auto load_tile = [&](u32x4 (&t)[8], const char* p, int stride, int wave_off, bool on) {
    const rsrc_t r = mk(p, on ? 0x7FFFFFFF : 0);
    const int off = wave_off + trow * stride + (lane & 7) * 16;
#pragma unroll
    for (int e = 0; e < 8; ++e) t[e] = __builtin_amdgcn_raw_buffer_load_b128(r, off, e * stride, WAUX);
  };
  const char* const p_wo = (const char*)a.wo + (g * 512 * 512 + (i & 7) * 64) * 2;
  const char* const p_wg = (const char*)a.wg + (is_mlp ? mi : 0) * 128;
  u32x4 t0[8], t1[8], t2[8];
  {
    const char* p = is_q ? (const char*)a.wq + (g * 512 + i * 64) * 2 : is_mod ? (const char*)a.wm + ((i - 16) * 512 + g * 64) * 2 : is_wo ? p_wo : p_wg;
    const int stride = is_q ? 8192 : is_mod ? 6144 : is_wo ? 1024 : 3584;
    load_tile(t0, p, stride, wave * 64 * stride, is_q || is_mod || (TW == 0 && (is_wo || is_mlp)));
  }

  u32x4 win[4][8];
  const int koff = ((lane >> 2) & 3) * 256 + ((lane >> 4) * 4 + (lane & 3)) * 16, voff = lane * 16;
  auto issue1 = [&](int blk, int n) {
    const int so = ((((blk & 7) + i) & 7) * 16 + (n >> 2) * 8 + wave) * 4096;
    if (blk < 8) win[blk & 3][n] = __builtin_amdgcn_raw_buffer_load_b128(rk, koff + (n & 3) * 1024, so, NT);
    else win[blk & 3][n] = __builtin_amdgcn_raw_buffer_load_b128(rv, voff + (n & 3) * 1024, so, NT);
  };
#define ISSUE(from, to) _Pragma("unroll") for (int p_ = (from); p_ < (to); ++p_) issue1(p_ >> 3, p_ & 7)
#define PACED(from, to) _Pragma("unroll") for (int p_ = (from); p_ < (to); ++p_) { \
    FENCE(); asm volatile("s_waitcnt vmcnt(%0)" :: "i"(PACE - 1) : "memory"); issue1(p_ >> 3, p_ & 7); FENCE(); }
  FENCE();
  ISSUE(0, PACE);
  FENCE();

  const unsigned long long tag = ((((unsigned long long)__builtin_amdgcn_readfirstlane(ep[1]) << 32) | (unsigned)__builtin_amdgcn_readfirstlane(ep[0])) + 1);
  const unsigned tag_lo = (unsigned)tag << 16, tag_hi = (unsigned)(tag >> 16);
  auto stale = [&](unsigned lo, unsigned hi) { return (hi != tag_hi) | ((lo & 0xffff0000u) != tag_lo); };
  unsigned modv = 0;
  if (is_mod) {
    auto silu = [](float v) { return v * rcp(1.f + __expf(-v)); };
    u32x4 w[8]; xpose(t0, w);
    put_red(l_red, wave, lane, tile_mv(w, pk(silu(blo(cpair)), silu(bhi(cpair)))));
    asm volatile("s_waitcnt vmcnt(0)" ::: "memory");
    bar();
    if (wave == 0) {
      const f32x2 r = fin_red(l_red, lane);
      modv = pk(r[0], r[1]);
      const u32x4 gr = {tag_lo | (modv & 0xffffu), tag_hi, tag_lo | (modv >> 16), tag_hi};
      __builtin_amdgcn_raw_buffer_store_b128(gr, rws, i < 18 ? WS_H + (((i - 16) * 2 + fb) * 512 + g * 64 + fc) * 8 : OOB, 0, SC1);
    }
    ST(1);
  }
  PACED(PACE, N1);
  if (is_q) {
    u32x4 w[8]; xpose(t0, w);
    float s0 = 0.f, s1 = 0.f;
#pragma unroll
    for (int e = 0; e < 4; ++e) { s0 += blo(xr0[e]) + bhi(xr0[e]); s1 += blo(xr1[e]) + bhi(xr1[e]); }
    const float m0 = wave_sum(s0) * (1.f / 512), m1 = wave_sum(s1) * (1.f / 512);
    float q0 = 0.f, q1 = 0.f;
#pragma unroll
    for (int e = 0; e < 4; ++e) {
      float d = blo(xr0[e]) - m0; q0 += d * d; d = bhi(xr0[e]) - m0; q0 += d * d;
      d = blo(xr1[e]) - m1; q1 += d * d; d = bhi(xr1[e]) - m1; q1 += d * d;
    }
    const float v0 = wave_sum(q0) * (1.f / 512), v1 = wave_sum(q1) * (1.f / 512);
    const float mean = rb(ab ? m1 : m0), isd = rcp(rb(__builtin_amdgcn_sqrtf(rb(rb(ab ? v1 : v0) + 1e-5f))));
    const int hoff = WS_H + (ab * 512 + kp) * 8;
    u32x4 sa, ca;
    unsigned sp = 0;
#pragma nounroll
    for (;;) {
      asm volatile("" ::: "memory");
      sa = __builtin_amdgcn_raw_buffer_load_b128(rws, hoff, 0, SC1); ca = __builtin_amdgcn_raw_buffer_load_b128(rws, hoff, 8192, SC1);
      if (__builtin_amdgcn_ballot_w64(stale(sa[0], sa[1]) | stale(sa[2], sa[3]) | stale(ca[0], ca[1]) | stale(ca[2], ca[3])) == 0 || ++sp >= SPIN_CAP) break;
      __builtin_amdgcn_s_sleep(2);
    }
    ST(2);
    put_red(l_red, wave, lane, tile_mv(w, pk(modulate(blo(xpair), mean, isd, blo(ca[0]), blo(sa[0])), modulate(bhi(xpair), mean, isd, blo(ca[2]), blo(sa[2])))));
    asm volatile("s_waitcnt vmcnt(0)" ::: "memory");
    bar();
    if (wave == 0) {
      const f32x2 r = fin_red(l_red, lane);
      const unsigned qp = pk(r[0], r[1]);
      const u32x4 gr = {tag_lo | (qp & 0xffffu), tag_hi, tag_lo | (qp >> 16), tag_hi};
      __builtin_amdgcn_raw_buffer_store_b128(gr, rws, WS_Q + ((g * 2 + fb) * 512 + i * 64 + fc) * 8, 0, SC1);
    }
    ST(3);
  }
  PACED(N1, N2);
  {
    const int off = WS_Q + ((g * 2 + b) * 512 + tid) * 8;
    u32x2 qg = __builtin_amdgcn_raw_buffer_load_b64(rws, off, 0, SC1);
    FENCE();
    ISSUE(N2, 32);
    FENCE();
    if (__builtin_amdgcn_ballot_w64(stale(qg[0], qg[1])) != 0) {
      unsigned sp = 0;
#pragma nounroll
      do {
        __builtin_amdgcn_s_sleep(2);
        asm volatile("" ::: "memory");
        qg = __builtin_amdgcn_raw_buffer_load_b64(rws, off, 0, SC1);
      } while (__builtin_amdgcn_ballot_w64(stale(qg[0], qg[1])) != 0 && ++sp < SPIN_CAP);
    }
    l_q[tid] = (unsigned short)qg[0];
  }
  bar();
  const u32x4 qf = *(const u32x4*)(l_q + (lane & 3) * 128 + ((lane >> 4) * 4 + ((lane >> 2) & 3)) * 8);
  __builtin_amdgcn_raw_buffer_store_b32(live(modv), rws, (is_mod && i >= 18 && wave == 0) ? WS_MOD + (((i - 18) * 2 + fb) * 512 + g * 64 + fc) * 2 : OOB, 0, SC1);
  ST(4);

  float S[16];
#pragma unroll
  for (int blk = 0; blk < 8; ++blk) {
#pragma unroll
    for (int t = 0; t < 2; ++t) {
      f32x4 d[4];
#pragma unroll
      for (int x = 0; x < 4; ++x)
        d[x] = __builtin_amdgcn_mfma_f32_16x16x32_bf16(__builtin_bit_cast(bf16x8, win[blk & 3][t * 4 + x]), __builtin_bit_cast(bf16x8, qf), f32x4{0.f, 0.f, 0.f, 0.f}, 0, 0, 0);
      const float t0 = dppb<QID, 8>(dppb<QID, 4>(dppb<QID, 2>(d[0][0], d[1][1]), d[2][2]), d[3][3]);
      const float t1 = dppb<ROR4, 8>(dppb<ROR4, 4>(dppb<ROR4, 2>(dpp<ROR4>(d[0][3]), d[1][0]), d[2][1]), d[3][2]);
      const float t2 = dppb<ROR8, 8>(dppb<ROR8, 4>(dppb<ROR8, 2>(dpp<ROR8>(d[0][2]), d[1][3]), d[2][0]), d[3][1]);
      const float t3 = dppb<ROR12, 8>(dppb<ROR12, 4>(dppb<ROR12, 2>(dpp<ROR12>(d[0][1]), d[1][2]), d[2][3]), d[3][0]);
      const float s = (t0 + t1) + (t2 + t3);
      S[blk * 2 + t] = RND_S ? rb(rb(s) * 0.08838834764831845f) : s * 0.08838834764831845f;
    }
    FENCE();
    ISSUE((blk + 4) * 8, (blk + 5) * 8);
    FENCE();
  }
  float mx = S[0];
#pragma unroll
  for (int u = 1; u < 16; ++u) mx = fmaxf(mx, S[u]);
  mx = fmaxf(mx, dpp<ROR4>(mx)); mx = fmaxf(mx, dpp<ROR8>(mx)); mx = rows_max(mx);
  if (lane < 4) l_mx[lane * 8 + wave] = mx;
  bar();
  ST(5);
  {
    const f32x4 m0 = *(const f32x4*)(l_mx + (lane & 3) * 8), m1 = *(const f32x4*)(l_mx + (lane & 3) * 8 + 4);
    mx = fmaxf(fmaxf(fmaxf(m0[0], m0[1]), fmaxf(m0[2], m0[3])), fmaxf(fmaxf(m1[0], m1[1]), fmaxf(m1[2], m1[3])));
  }
  constexpr float LOG2E = 1.4426950408889634f;
  unsigned P[8];
  {
    float ls = 0.f;
#pragma unroll
    for (int u = 0; u < 8; ++u) {
      const float p0 = __builtin_amdgcn_exp2f((S[2 * u] - mx) * LOG2E), p1 = __builtin_amdgcn_exp2f((S[2 * u + 1] - mx) * LOG2E);
      ls += p0 + p1;
      P[u] = pk(p0, p1);
    }
    ls += dpp<ROR4>(ls); ls += dpp<ROR8>(ls); ls = sw16(ls, ls); ls = sw32(ls, ls);
    if (lane < 4) l_ls[lane * 8 + wave] = ls;
  }
  f32x4 acc[8];
#pragma unroll
  for (int j = 0; j < 8; ++j) acc[j] = f32x4{0.f, 0.f, 0.f, 0.f};
  unsigned gap, sbp, cbp, gbp;
  const int ob = (mi >> 3) & 1, ocol = ((mi & 7) * 8 + wave) * 8 + (lane & 1) * 4 + (lane >> 5) * 2;
  auto load_mod = [&]() {
    const int off = is_mlp ? WS_MOD + (ab * 512 + kp) * 2 : OOB;
    gap = __builtin_amdgcn_raw_buffer_load_b32(rws, off, 0, SC1);
    sbp = __builtin_amdgcn_raw_buffer_load_b32(rws, off, 2048, SC1);
    cbp = __builtin_amdgcn_raw_buffer_load_b32(rws, off, 4096, SC1);
    gbp = __builtin_amdgcn_raw_buffer_load_b32(rws, is_mlp ? WS_MOD + (ob * 512 + ocol) * 2 : OOB, 6144, SC1);
  };
#pragma unroll
  for (int blk = 8; blk < 16; ++blk) {
    const unsigned p = P[blk - 8];
    const u32x4 pb = {p, dppu<SHL4>(p), dppu<SHL8>(p), dppu<SHL12>(p)};
#pragma unroll
    for (int j = 0; j < 8; ++j) {
      u32x4 va;
#pragma unroll
      for (int x = 0; x < 4; ++x) va[x] = __builtin_amdgcn_perm(win[blk & 3][4 + x][j >> 1], win[blk & 3][x][j >> 1], (j & 1) ? 0x07060302 : 0x05040100);
      acc[j] = __builtin_amdgcn_mfma_f32_16x16x32_bf16(__builtin_bit_cast(bf16x8, va), __builtin_bit_cast(bf16x8, pb), acc[j], 0, 0, 0);
    }
    FENCE();
    if (blk < 12) { ISSUE((blk + 4) * 8, (blk + 5) * 8); }
    if (blk == 12) {
      load_mod();
      if (TW == 1) load_tile(t0, is_wo ? p_wo : p_wg, is_wo ? 1024 : 3584, wave * 64 * (is_wo ? 1024 : 3584), is_wo || is_mlp);
    }
    if (blk == 12 + TW) load_tile(t1, (const char*)a.wu + (is_mlp ? mi : 0) * 128, 3584, wave * 64 * 3584, is_mlp);
    if (blk == 13 + TW) load_tile(t2, (const char*)a.wd + (is_mlp ? mi : 0) * 65536, 1024, wave * 128, is_mlp);
    FENCE();
  }
  ST(6);
  if ((lane & 12) == 0) {
#pragma unroll
    for (int j = 0; j < 8; ++j) *(f32x4*)(l_o + wave * 512 + (lane & 3) * 128 + (lane >> 4) * 32 + j * 4) = acc[j];
  }
  bar();
  if (wave < 4) {
    const float* p = l_o + (lane >> 4) * 1024 + wave * 128 + (lane & 15) * 8;
    const f32x4 e = *(const f32x4*)p + *(const f32x4*)(p + 512), o = *(const f32x4*)(p + 4) + *(const f32x4*)(p + 516);
    const f32x4 l0 = *(const f32x4*)(l_ls + wave * 8), l1 = *(const f32x4*)(l_ls + wave * 8 + 4);
    const float l = ((l0[0] + l0[1]) + (l0[2] + l0[3])) + ((l1[0] + l1[1]) + (l1[2] + l1[3]));
    const float inv = rcp(l);
    const float ee = sw16(sw32(e[0], e[2]), sw32(e[1], e[3])), oo = sw16(sw32(o[0], o[2]), sw32(o[1], o[3]));
    const int base = WS_O + (g * 32 + i) * O_STRIDE;
    __builtin_amdgcn_raw_buffer_store_b32(live(pk(ee * inv, oo * inv)), rws, base + wave * 256 + (lane & 15) * 16 + (lane >> 4) * 4, 0, SC1);
    const u32x2 ml = {live(asu(mx)), live(asu(l))};
    __builtin_amdgcn_raw_buffer_store_b64(ml, rws, lane == wave ? base + 1024 + wave * 8 : OOB, 0, SC1);
  }
  ST(7);
  if (is_wo) {
    u32x4 w[8]; xpose(t0, w);
    const int src = WS_O + (g * 32 + ab * 16 + (lane >> 4)) * O_STRIDE;
    const int ooff = src + wave * 128 + (((lane >> 3) & 1) * 4 + (lane & 3)) * 16, loff = src + 1024 + (wave >> 1) * 8;
    u32x4 ov[4]; u32x2 ml[4];
    bool need[4] = {true, true, true, true};
#pragma nounroll
    for (unsigned sp = 0; sp < SPIN_CAP; ++sp) {
      asm volatile("" ::: "memory");
#pragma unroll
      for (int u = 0; u < 4; ++u) if (need[u]) {
        ov[u] = __builtin_amdgcn_raw_buffer_load_b128(rws, ooff, u * 4 * O_STRIDE, SC1);
        ml[u] = __builtin_amdgcn_raw_buffer_load_b64(rws, loff, u * 4 * O_STRIDE, SC1);
      }
#pragma unroll
      for (int u = 0; u < 4; ++u) need[u] = __builtin_amdgcn_ballot_w64((ov[u][0] == EMPTY) | (ov[u][1] == EMPTY) | (ov[u][2] == EMPTY) | (ov[u][3] == EMPTY) | (ml[u][0] == EMPTY) | (ml[u][1] == EMPTY)) != 0;
      if (!(need[0] | need[1] | need[2] | need[3])) break;
      __builtin_amdgcn_s_sleep(1);
    }
    ST(8);
    const float M = rows_max(fmaxf(fmaxf(asf(ml[0][0]), asf(ml[1][0])), fmaxf(asf(ml[2][0]), asf(ml[3][0]))));
    float wt[4], L = 0.f;
#pragma unroll
    for (int u = 0; u < 4; ++u) { wt[u] = __builtin_amdgcn_exp2f((asf(ml[u][0]) - M) * LOG2E) * asf(ml[u][1]); L += wt[u]; }
    L = sw16(L, L); L = sw32(L, L);
    float e[4], o[4];
#pragma unroll
    for (int r = 0; r < 4; ++r) {
      e[r] = wt[0] * blo(ov[0][r]); o[r] = wt[0] * bhi(ov[0][r]);
#pragma unroll
      for (int u = 1; u < 4; ++u) { e[r] = fmaf(wt[u], blo(ov[u][r]), e[r]); o[r] = fmaf(wt[u], bhi(ov[u][r]), o[r]); }
    }
    const float inv = rcp(L);
    put_red(l_red, wave, lane, tile_mv(w, pk(sw16(sw32(e[0], e[2]), sw32(e[1], e[3])) * inv, sw16(sw32(o[0], o[2]), sw32(o[1], o[3])) * inv)));
    asm volatile("s_waitcnt vmcnt(0)" ::: "memory");
    bar();
    if (wave == 0) {
      const f32x2 r = fin_red(l_red, lane);
      const int piece = ((fc >> 3) & 1) * 8 + (fc >> 5) * 4 + ((fc >> 1) & 3);
      const u32x2 yv = {live(asu(r[0])), live(asu(r[1]))};
      __builtin_amdgcn_raw_buffer_store_b64(yv, rws, WS_Y + (((g * 2 + fb) * 8 + (i & 7)) * 64 + piece * 4 + ((fc >> 4) & 1) * 2) * 4, 0, SC1);
    }
    ST(9);
  }
  if (is_mlp) {
    u32x4 wg[8], wu[8], wd[8]; xpose(t0, wg); xpose(t1, wu); xpose(t2, wd);
#pragma nounroll
    for (unsigned sp = 0; sp < SPIN_CAP; ++sp) {
      if (__builtin_amdgcn_ballot_w64((gap == EMPTY) | (sbp == EMPTY) | (cbp == EMPTY) | (gbp == EMPTY)) == 0) break;
      asm volatile("" ::: "memory");
      load_mod();
    }
    const int yoff = WS_Y + ((((lane >> 5) * 8 + ab) * 8 + wave) * 64 + (((lane >> 3) & 3) * 4 + (lane & 3)) * 4) * 4;
    u32x4 yv[4];
    bool need[4] = {true, true, true, true};
#pragma nounroll
    for (unsigned sp = 0; sp < SPIN_CAP; ++sp) {
      asm volatile("" ::: "memory");
#pragma unroll
      for (int u = 0; u < 4; ++u) if (need[u]) yv[u] = __builtin_amdgcn_raw_buffer_load_b128(rws, yoff, u * 4096, SC1);
#pragma unroll
      for (int u = 0; u < 4; ++u) need[u] = any_empty(yv[u]);
      if (!(need[0] | need[1] | need[2] | need[3])) break;
      __builtin_amdgcn_s_sleep(1);
    }
    ST(10);
    float y[4];
#pragma unroll
    for (int r = 0; r < 4; ++r) y[r] = (asf(yv[0][r]) + asf(yv[1][r])) + (asf(yv[2][r]) + asf(yv[3][r]));
    const float xa = rb(blo(xpair) + rb(blo(gap) * rb(sw32(y[0], y[2])))), xb = rb(bhi(xpair) + rb(bhi(gap) * rb(sw32(y[1], y[3]))));
    l_x1[ab * 256 + (kp >> 1)] = pk(xa, xb);
    const float mw = sum_sameb(xa + xb) * (1.f / 64);
    const float m2 = sum_sameb((xa - mw) * (xa - mw) + (xb - mw) * (xb - mw));
    if ((lane & 59) == 0) *(f32x2*)(l_st + ab * 16 + wave * 2) = f32x2{mw, m2};
    bar();
    f32x4 st[4];
#pragma unroll
    for (int u = 0; u < 4; ++u) st[u] = *(const f32x4*)(l_st + ab * 16 + u * 4);
    const float mean = (((st[0][0] + st[0][2]) + (st[1][0] + st[1][2])) + ((st[2][0] + st[2][2]) + (st[3][0] + st[3][2]))) * 0.125f;
    float dd = 0.f, mm = 0.f;
#pragma unroll
    for (int u = 0; u < 4; ++u) { const float d0 = st[u][0] - mean, d1 = st[u][2] - mean; dd += d0 * d0 + d1 * d1; mm += st[u][1] + st[u][3]; }
    const float isd = rcp(rb(__builtin_amdgcn_sqrtf(rb(rb((mm + 64.f * dd) * (1.f / 512)) + 1e-5f)))), mb = rb(mean);
    const unsigned hp = pk(modulate(xa, mb, isd, blo(cbp), blo(sbp)), modulate(xb, mb, isd, bhi(cbp), bhi(sbp)));
    put_red(l_red, wave, lane, tile_mv(wg, hp));
    put_red(l_red + 1024, wave, lane, tile_mv(wu, hp));
    bar();
    f32x2 gs = {0.f, 0.f}, us = {0.f, 0.f};
#pragma unroll
    for (int v = 0; v < 8; ++v) { const float* p = l_red + v * 128 + ab * 64 + (kp & 63); gs += *(const f32x2*)p; us += *(const f32x2*)(p + 1024); }
    auto geglu = [](float gv, float uv) {
      gv = rb(gv);
      const float th = 1.f - 2.f * rcp(1.f + __expf(2.f * 0.7978845608028654f * (gv + 0.044715f * gv * gv * gv)));
      return rb(0.5f * gv * (1.f + th)) * rb(uv);
    };
    const f32x4 z = tile_mv(wd, pk(geglu(gs[0], us[0]), geglu(gs[1], us[1])));
    const u32x4 zo = {live(asu(z[0])), live(asu(z[1])), live(asu(z[2])), live(asu(z[3]))};
    __builtin_amdgcn_raw_buffer_store_b128(zo, rws, (lane & 8) ? OOB : WS_Z + ((mi * 2 + fb) * 512 + wave * 64 + (lane & 7) * 8 + (lane >> 5) * 4) * 4, 0, SC1);
    ST(11);
    if (mi < 16) {
      const int zoff = lane < 56 ? WS_Z + (((lane >> 1) * 2 + ob) * 512 + ((mi & 7) * 8 + wave) * 8 + (lane & 1) * 4) * 4 : OOB;
      const unsigned xp = l_x1[ob * 256 + (ocol >> 1)];
      u32x4 zv;
#pragma nounroll
      for (unsigned sp = 0; sp < SPIN_CAP; ++sp) {
        asm volatile("" ::: "memory");
        zv = __builtin_amdgcn_raw_buffer_load_b128(rws, zoff, 0, SC1);
        if (!any_empty(zv)) break;
        __builtin_amdgcn_s_sleep(1);
      }
      ST(12);
      float z0 = sw32(asf(zv[0]), asf(zv[2])), z1 = sw32(asf(zv[1]), asf(zv[3]));
      z0 = sw16(z0, z0); z1 = sw16(z1, z1);
      z0 += dpp<ROR8>(z0); z1 += dpp<ROR8>(z1); z0 += dpp<ROR4>(z0); z1 += dpp<ROR4>(z1); z0 += dpp<XOR2>(z0); z1 += dpp<XOR2>(z1);
      const unsigned op = pk(blo(xp) + rb(blo(gbp) * rb(z0)), bhi(xp) + rb(bhi(gbp) * rb(z1)));
      if ((lane & 30) == 0) *(unsigned*)((char*)a.out + (ob * 512 + ocol) * 2) = op;
      if (mi == 0 && tid == 0) { const u32x2 ne = {(unsigned)tag, (unsigned)(tag >> 32)}; __builtin_amdgcn_raw_buffer_store_b64(ne, rws, WS_EPOCH, 0, SC1); }
      ST(13);
    }
  }
}

extern "C" void kernel_launch(void* const* d_in, const int* in_sizes, int n_in, void* d_out, int out_size, void* d_ws, size_t ws_size, hipStream_t stream) {
  Args a;
  a.x = d_in[0]; a.k = d_in[1]; a.v = d_in[2]; a.wq = d_in[3]; a.wo = d_in[4];
  a.wg = d_in[6]; a.wu = d_in[7]; a.wd = d_in[8]; a.c = d_in[9]; a.wm = d_in[10];
  a.out = d_out; a.ws = d_ws;
  fused_block<<<256, 512, 0, stream>>>(a);
}
```

```cpp
#include <hip/hip_runtime.h>
#include <hip/hip_bf16.h>
#include <stdint.h>

#define PACE 3
#define N1 4
#define N2 26
#define TW 1
#define QSKIP 1
#define ROT 1
#define DRY 7
#define DQ 21
#define DWO 12
#define DMOD 10
#define DMLP 26
#define DPAR 75
#define RMAP 1
#define SLP 1
#define PRE 1
#define KAUX 2
#define VAUX 2
#define OAUX 0
#define WAUX 2
#define RND_S 1

using u32x2 = __attribute__((ext_vector_type(2))) unsigned;
using u32x4 = __attribute__((ext_vector_type(4))) unsigned;
using f32x4 = __attribute__((ext_vector_type(4))) float;
using bf16x2 = __attribute__((ext_vector_type(2))) __bf16;
using bf16x8 = __attribute__((ext_vector_type(8))) __bf16;
using f32x2 = __attribute__((ext_vector_type(2))) float;
typedef __amdgpu_buffer_rsrc_t rsrc_t;

constexpr int SC1 = 16, NT = 2;
constexpr int WS_EPOCH = 0;
#define RS 4352
constexpr int WS_H = RS;
constexpr int WS_Q = WS_H + 32 * RS;
constexpr int WS_Y = WS_Q + 128 * RS;
constexpr int WS_MOD = WS_Y + 64 * RS;
constexpr int WS_Z = WS_MOD + 8192;
constexpr int ZS = RS > 4096 ? RS : 4096;
constexpr int WS_O = WS_Z + 32 * ZS;
constexpr int WS_BYTES = WS_O + 256 * RS;
constexpr unsigned EMPTY = 0xFFFFFFFFu;
constexpr unsigned SPIN_CAP = 1u << 18;
constexpr int FAR = 0x40000000, OOB = 0x7FFFFFF0;

struct Ptrs {
  const void *x, *k, *v, *wq, *wo, *wg, *wu, *wd, *c, *wm;
  void *out, *ws;
};
struct Args {
  Ptrs p;
  unsigned share[512];
};

__host__ __device__ __forceinline__ int own_piece(int i, int slot, int wave) {
  const int rot = ROT == 1 ? i >> 1 : ROT == 2 ? i : ROT == 3 ? (i >> 2) : (i >> 2) * 2 + (i & 1);
  return (((i >> 2) << 1) | (i & 1)) * 128 + (((slot >> 1) + rot) & 7) * 16 + (slot & 1) * 8 + wave;
}
__host__ __device__ __forceinline__ int role_of(int pos) {
  const int j = pos >> 1;
  return !RMAP ? pos : (pos & 1) ? j + (j < 6 ? 10 : j < 10 ? 12 : 16) : j + (j < 8 ? 0 : j < 12 ? 14 : j < 14 ? 4 : -6);
}
__device__ __forceinline__ rsrc_t mk(const void* p, unsigned bytes) { return __builtin_amdgcn_make_buffer_rsrc((void*)p, 0, bytes, 0x00020000); }
__device__ __forceinline__ float asf(unsigned u) { return __builtin_bit_cast(float, u); }
__device__ __forceinline__ unsigned asu(float f) { return __builtin_bit_cast(unsigned, f); }
__device__ __forceinline__ float blo(unsigned u) { return asf(u << 16); }
__device__ __forceinline__ float bhi(unsigned u) { return asf(u & 0xffff0000u); }
__device__ __forceinline__ unsigned pk(float lo, float hi) {
  f32x2 v = {lo, hi};
  return __builtin_bit_cast(unsigned, __builtin_convertvector(v, bf16x2));
}
__device__ __forceinline__ float rb(float x) { return (float)(__bf16)x; }
__device__ __forceinline__ unsigned live(unsigned u) { return min(u, EMPTY - 1); }
__device__ __forceinline__ float dot2(unsigned a, unsigned b, float c) {
  return __builtin_amdgcn_fdot2_f32_bf16(__builtin_bit_cast(bf16x2, a), __builtin_bit_cast(bf16x2, b), c, false);
}
template <int CTRL> __device__ __forceinline__ float dpp(float x) { return asf(__builtin_amdgcn_mov_dpp(asu(x), CTRL, 0xf, 0xf, true)); }
template <int CTRL> __device__ __forceinline__ unsigned dppu(unsigned x) { return __builtin_amdgcn_mov_dpp(x, CTRL, 0xf, 0xf, true); }
template <int CTRL, int BANK> __device__ __forceinline__ float dppb(float old, float x) {
  return asf(__builtin_amdgcn_update_dpp(asu(old), asu(x), CTRL, 0xf, BANK, false));
}
constexpr int QID = 0xE4, XOR1 = 0xB1, XOR2 = 0x4E, SHL4 = 0x104, SHL8 = 0x108, SHL12 = 0x10C, SHR4 = 0x114, ROR4 = 0x124, ROR8 = 0x128, ROR12 = 0x12C;
__device__ __forceinline__ float sw32(float a, float b) {
  auto s = __builtin_amdgcn_permlane32_swap(asu(a), asu(b), false, false);
  return asf(s[0]) + asf(s[1]);
}
__device__ __forceinline__ float sw16(float a, float b) {
  auto s = __builtin_amdgcn_permlane16_swap(asu(a), asu(b), false, false);
  return asf(s[0]) + asf(s[1]);
}
__device__ __forceinline__ float rows_max(float x) {
  auto s = __builtin_amdgcn_permlane16_swap(asu(x), asu(x), false, false);
  x = fmaxf(asf(s[0]), asf(s[1]));
  auto t = __builtin_amdgcn_permlane32_swap(asu(x), asu(x), false, false);
  return fmaxf(asf(t[0]), asf(t[1]));
}
__device__ __forceinline__ float wave_sum(float x) {
  x += dpp<XOR1>(x); x += dpp<XOR2>(x); x += dpp<ROR4>(x); x += dpp<ROR8>(x);
  x = sw16(x, x);
  return sw32(x, x);
}
__device__ __forceinline__ void bar() { asm volatile("s_waitcnt lgkmcnt(0)\n\ts_barrier" ::: "memory"); }
__device__ __forceinline__ int opaque(int v) { asm volatile("" : "+s"(v)); return v; }
#define PASSES(dry) _Pragma("nounroll") for (int pass = opaque(__builtin_amdgcn_readfirstlane((dry) ? 0 : 1)); pass < 2; pass = opaque(pass + 1))
template <class T> __device__ __forceinline__ void anyval(T& v) { asm volatile("" : "=v"(v)); }
#define FENCE() __builtin_amdgcn_sched_barrier(0)
__device__ __forceinline__ void xpose(const u32x4 (&t)[8], u32x4 (&o)[8]) {
#pragma unroll
  for (int j = 0; j < 8; ++j)
#pragma unroll
    for (int e = 0; e < 4; ++e) o[j][e] = __builtin_amdgcn_perm(t[2 * e + 1][j >> 1], t[2 * e][j >> 1], (j & 1) ? 0x07060302 : 0x05040100);
}
__device__ __forceinline__ f32x4 tile_mv(const u32x4 (&w)[8], unsigned ap) {
  const u32x4 av = {dppu<0x00>(ap), dppu<0x55>(ap), dppu<0xAA>(ap), dppu<0xFF>(ap)};
  float u[8];
#pragma unroll
  for (int j = 0; j < 8; ++j) {
    const f32x4 d = __builtin_amdgcn_mfma_f32_16x16x32_bf16(__builtin_bit_cast(bf16x8, av), __builtin_bit_cast(bf16x8, w[j]), f32x4{0.f, 0.f, 0.f, 0.f}, 0, 0, 0);
    u[j] = asf(__builtin_amdgcn_update_dpp(asu(d[0]), asu(d[0]), ROR8, 0xC, 0xf, false));
  }
  return f32x4{sw32(u[0], u[4]), sw32(u[1], u[5]), sw32(u[2], u[6]), sw32(u[3], u[7])};
}
__device__ __forceinline__ void put_red(float* red, int wave, int lane, const f32x4& r) {
  if (!(lane & 8)) *(f32x4*)(red + wave * 128 + ((lane >> 4) & 1) * 64 + (lane & 7) * 8 + (lane >> 5) * 4) = r;
}
__device__ __forceinline__ f32x2 fin_red(const float* red, int lane) {
  const float* p = red + (lane >> 5) * 512 + (lane & 31) * 4;
  const f32x4 v = (*(const f32x4*)p + *(const f32x4*)(p + 128)) + (*(const f32x4*)(p + 256) + *(const f32x4*)(p + 384));
  return f32x2{sw32(v[0], v[2]), sw32(v[1], v[3])};
}
__device__ __forceinline__ float sum_sameb(float x) {
  x += dpp<XOR1>(x); x += dpp<XOR2>(x); x += dpp<ROR8>(x);
  x = sw16(x, x);
  return sw32(x, x);
}
__device__ __forceinline__ float rcp(float x) { return __builtin_amdgcn_rcpf(x); }
__device__ __forceinline__ float modulate(float x, float mean, float isd, float c, float s) { return rb(rb(rb(x - mean) * isd) * rb(1.f + c)) + s; }
__device__ __forceinline__ unsigned umax3(unsigned x, unsigned y, unsigned z) { return max(max(x, y), z); }
__device__ __forceinline__ bool any_empty(const u32x4& v) { return __builtin_amdgcn_ballot_w64(umax3(umax3(v[0], v[1], v[2]), v[3], 0) == EMPTY) != 0; }
__device__ __forceinline__ void pre_poll(rsrc_t r, int off, int pass) {
#pragma nounroll
  for (unsigned sp = pass ? 0 : SPIN_CAP; sp < SPIN_CAP; ++sp) {
    asm volatile("" ::: "memory");
    if (__builtin_amdgcn_readfirstlane(__builtin_amdgcn_raw_buffer_load_b32(r, 0, off, SC1)) != EMPTY) break;
    __builtin_amdgcn_s_sleep(2);
  }
}

#define ST(n)
#define FST(n)

__global__ void __launch_bounds__(512, 2) fused_block(Args args) {
  __shared__ __attribute__((aligned(16))) float lds[4096 + 2048 + 64 + 512 + 32 + 256];
  float* const l_o = lds;
  float* const l_red = lds + 4096;
  float* const l_mx = l_red + 2048;
  float* const l_ls = l_mx + 32;
  unsigned* const l_x1 = (unsigned*)(l_ls + 32);
  float* const l_st = (float*)(l_x1 + 512);
  unsigned short* const l_q = (unsigned short*)(l_st + 32);
  const int tid = threadIdx.x, lane = tid & 63, wave = __builtin_amdgcn_readfirstlane(tid >> 6);
  ST(0); FENCE();
  const int bid = blockIdx.x, g = (bid & 6) | ((bid >> 3) & 1), pos = ((bid >> 4) << 1) | (bid & 1), b = (pos >> 1) & 1, split = ((pos >> 2) << 1) | (pos & 1);
  const int i = role_of(pos);
  Ptrs a = args.p;
  unsigned share = args.share[((g >> 2) * 32 + pos) * 8 + wave];
  asm volatile("" : "+s"(a.x), "+s"(a.k), "+s"(a.v), "+s"(a.wq), "+s"(a.wo), "+s"(a.wg), "+s"(a.wu), "+s"(a.wd), "+s"(a.c), "+s"(a.wm), "+s"(a.out), "+s"(a.ws), "+s"(share));
  ST(15);
  const rsrc_t rws = mk(a.ws, WS_BYTES);
  const size_t kvbase = (size_t)(b * 8 + g) << 23;
  const rsrc_t rk = mk((const char*)a.k + kvbase, 1 << 23), rv = mk((const char*)a.v + kvbase, 1 << 23);
  const int mi = (i - 22) * 8 + g;
  const bool is_q = i < 8, is_wo = i >= 8 && i < 16, is_mod = i >= 16 && i < 22, is_mlp = i >= 22 && mi < 28;
  const int trow = ((lane >> 3) & 1) * 32 + (lane >> 4) * 8;
  const int ab = (lane >> 2) & 1, kp = wave * 64 + trow + (lane & 3) * 2;
  const int fb = (lane >> 4) & 1, fc = (lane & 15) * 4 + (lane >> 5) * 2;

  const u32x2 ep = __builtin_amdgcn_raw_buffer_load_b64(rws, WS_EPOCH, 0, SC1);
  {
    const u32x4 e4 = {EMPTY, EMPTY, EMPTY, EMPTY};
    const int r = g * 8 + (i & 7), hr = g * 2 + (i & 1), t = tid & 127;
    const int cbase = is_q ? WS_O + (r * 4 + (wave >> 1)) * RS : is_wo ? WS_Z + (r >> 1) * ZS + (r & 1) * 2048 : WS_Y + (hr * 4 + (wave >> 1)) * RS;
    const int cn = is_q ? 72 : is_wo ? (wave < 2 ? 128 : 0) : (i == 16 || i == 17) ? 32 : 0;
    __builtin_amdgcn_raw_buffer_store_b128(e4, rws, t < cn ? cbase + t * 16 : (cn == 32 && tid < 64) ? WS_MOD + hr * 512 + (tid - 32) * 16 : OOB, 0, SC1);
  }
  const rsrc_t rc = mk(a.c, 2048), rx = mk(a.x, 2048);
  const unsigned cpair = __builtin_amdgcn_raw_buffer_load_b32(rc, (ab * 512 + kp) * 2, 0, 0);
  const unsigned xpair = __builtin_amdgcn_raw_buffer_load_b32(rx, (ab * 512 + kp) * 2, 0, 0);
  const u32x4 xr0 = __builtin_amdgcn_raw_buffer_load_b128(rx, lane * 16, 0, 0), xr1 = __builtin_amdgcn_raw_buffer_load_b128(rx, lane * 16 + 1024, 0, 0);
  auto load_tile = [&](u32x4 (&t)[8], const char* p, int stride, int wave_off, bool on) {
    const rsrc_t r = mk(p, on ? 0x7FFFFFFF : 0);
    const int off = wave_off + trow * stride + (lane & 7) * 16;
#pragma unroll
    for (int e = 0; e < 8; ++e) t[e] = __builtin_amdgcn_raw_buffer_load_b128(r, off, e * stride, WAUX);
  };
  const char* const p_wo = (const char*)a.wo + (g * 512 * 512 + (i & 7) * 64) * 2;
  const char* const p_wg = (const char*)a.wg + (is_mlp ? mi : 0) * 128;
  u32x4 t0[8], t1[8], t2[8];
  {
    const char* p = is_q ? (const char*)a.wq + (g * 512 + i * 64) * 2 : is_mod ? (const char*)a.wm + ((i - 16) * 512 + g * 64) * 2 : is_wo ? p_wo : p_wg;
    const int stride = is_q ? 8192 : is_mod ? 6144 : is_wo ? 1024 : 3584;
    load_tile(t0, p, stride, wave * 64 * stride, is_q || is_mod || (TW == 0 && (is_wo || is_mlp)));
  }

  u32x4 win[4][8];
  const int koff = ((lane >> 2) & 3) * 256 + ((lane >> 4) * 4 + (lane & 3)) * 16, voff = lane * 16;
  auto piece = [&](int blk, int t) {
    const int u = blk % 9, own = own_piece(pos, u * 2 + t, wave);
    return u == 8 ? (int)(share >> (12 * t)) & 0xFFF : u < 5 || u * 2 + t < (int)(share >> 24) ? own : 0xFFF;
  };
  auto issue1 = [&](int blk, int n) {
    const int so = piece(blk, n >> 2) * 4096;
    if (blk < 9) win[blk & 3][n] = __builtin_amdgcn_raw_buffer_load_b128(rk, koff + (n & 3) * 1024, so, KAUX);
    else win[blk & 3][n] = __builtin_amdgcn_raw_buffer_load_b128(rv, voff + (n & 3) * 1024, so, VAUX);
  };
#define ISSUE(from, to) _Pragma("unroll") for (int p_ = (from); p_ < (to); ++p_) issue1(p_ >> 3, p_ & 7)
#define PACED(from, to, skip) _Pragma("unroll") for (int p_ = (from); p_ < (to); ++p_) { \
    FENCE(); asm volatile("s_cmp_lt_u32 %1, %2\n\ts_cbranch_scc1 1f\n\ts_waitcnt vmcnt(%0)\n1:" :: "i"(PACE - 1), "s"(i), "i"(skip) : "memory", "scc"); issue1(p_ >> 3, p_ & 7); FENCE(); }
  FENCE();
  ISSUE(0, PACE);
  FENCE();

  const unsigned long long tag = ((((unsigned long long)__builtin_amdgcn_readfirstlane(ep[1]) << 32) | (unsigned)__builtin_amdgcn_readfirstlane(ep[0])) + 1);
  const unsigned tag_lo = (unsigned)tag << 16, tag_hi = (unsigned)(tag >> 16);
  auto stale = [&](unsigned lo, unsigned hi) { return (hi != tag_hi) | ((lo & 0xffff0000u) != tag_lo); };
  unsigned modv = 0;
  if (is_mod) {
    auto silu = [](float v) { return v * rcp(1.f + __expf(-v)); };
    u32x4 w[8]; xpose(t0, w);
    put_red(l_red, wave, lane, tile_mv(w, pk(silu(blo(cpair)), silu(bhi(cpair)))));
    asm volatile("s_waitcnt vmcnt(0)" ::: "memory");
    bar();
    if (wave == 0) {
      const f32x2 r = fin_red(l_red, lane);
      modv = pk(r[0], r[1]);
      const u32x4 gr = {tag_lo | (modv & 0xffffu), tag_hi, tag_lo | (modv >> 16), tag_hi};
      __builtin_amdgcn_raw_buffer_store_b128(gr, rws, i < 18 ? WS_H + (((i - 16) * 2 + fb) * 8 + g) * RS + fc * 8 : OOB, 0, SC1);
    }
    ST(1);
  }
  PACED(PACE, N1, 0);
  if (is_q) {
    u32x4 w[8]; xpose(t0, w);
    float s0 = 0.f, s1 = 0.f;
#pragma unroll
    for (int e = 0; e < 4; ++e) { s0 += blo(xr0[e]) + bhi(xr0[e]); s1 += blo(xr1[e]) + bhi(xr1[e]); }
    const float m0 = wave_sum(s0) * (1.f / 512), m1 = wave_sum(s1) * (1.f / 512);
    float q0 = 0.f, q1 = 0.f;
#pragma unroll
    for (int e = 0; e < 4; ++e) {
      float d = blo(xr0[e]) - m0; q0 += d * d; d = bhi(xr0[e]) - m0; q0 += d * d;
      d = blo(xr1[e]) - m1; q1 += d * d; d = bhi(xr1[e]) - m1; q1 += d * d;
    }
    const float v0 = wave_sum(q0) * (1.f / 512), v1 = wave_sum(q1) * (1.f / 512);
    const float mean = rb(ab ? m1 : m0), isd = rcp(rb(__builtin_amdgcn_sqrtf(rb(rb(ab ? v1 : v0) + 1e-5f))));
    const int hoff = WS_H + (ab * 8 + wave) * RS + (kp & 63) * 8;
    u32x4 sa, ca;
    unsigned sp = 0;
#pragma nounroll
    for (;;) {
      asm volatile("" ::: "memory");
      sa = __builtin_amdgcn_raw_buffer_load_b128(rws, hoff, 0, SC1); ca = __builtin_amdgcn_raw_buffer_load_b128(rws, hoff, 16 * RS, SC1);
      if (__builtin_amdgcn_ballot_w64(stale(sa[0], sa[1]) | stale(sa[2], sa[3]) | stale(ca[0], ca[1]) | stale(ca[2], ca[3])) == 0 || ++sp >= SPIN_CAP) break;
      __builtin_amdgcn_s_sleep(2);
    }
    ST(2);
    put_red(l_red, wave, lane, tile_mv(w, pk(modulate(blo(xpair), mean, isd, blo(ca[0]), blo(sa[0])), modulate(bhi(xpair), mean, isd, blo(ca[2]), blo(sa[2])))));
    asm volatile("s_waitcnt vmcnt(0)" ::: "memory");
    bar();
    if (wave == 0) {
      const f32x2 r = fin_red(l_red, lane);
      const unsigned qp = pk(r[0], r[1]);
      const u32x4 gr = {tag_lo | (qp & 0xffffu), tag_hi, tag_lo | (qp >> 16), tag_hi};
      __builtin_amdgcn_raw_buffer_store_b128(gr, rws, WS_Q + ((g * 2 + fb) * 8 + i) * RS + fc * 8, 0, SC1);
    }
    ST(3);
  }
  PACED(N1, N2, QSKIP * 8);
  {
    const int off = WS_Q + ((g * 2 + b) * 8 + wave) * RS + lane * 8;
    u32x2 qg = __builtin_amdgcn_raw_buffer_load_b64(rws, off, 0, SC1);
    FENCE();
    ISSUE(N2, 32);
    FENCE();
    if (__builtin_amdgcn_ballot_w64(stale(qg[0], qg[1])) != 0) {
      unsigned sp = 0;
#pragma nounroll
      do {
        __builtin_amdgcn_s_sleep(2);
        asm volatile("" ::: "memory");
        qg = __builtin_amdgcn_raw_buffer_load_b64(rws, off, 0, SC1);
      } while (__builtin_amdgcn_ballot_w64(stale(qg[0], qg[1])) != 0 && ++sp < SPIN_CAP);
    }
    l_q[tid] = (unsigned short)qg[0];
  }
  bar();
  const u32x4 qf = *(const u32x4*)(l_q + (lane & 3) * 128 + ((lane >> 4) * 4 + ((lane >> 2) & 3)) * 8);
  __builtin_amdgcn_raw_buffer_store_b32(live(modv), rws, (is_mod && i >= 18 && wave == 0) ? WS_MOD + (((i - 18) * 2 + fb) * 512 + g * 64 + fc) * 2 : OOB, 0, SC1);
  ST(4);

  float S[18];
#pragma unroll
  for (int blk = 0; blk < 9; ++blk) {
#pragma unroll
    for (int t = 0; t < 2; ++t) {
      f32x4 d[4];
#pragma unroll
      for (int x = 0; x < 4; ++x)
        d[x] = __builtin_amdgcn_mfma_f32_16x16x32_bf16(__builtin_bit_cast(bf16x8, win[blk & 3][t * 4 + x]), __builtin_bit_cast(bf16x8, qf), f32x4{0.f, 0.f, 0.f, 0.f}, 0, 0, 0);
      const float t0 = dppb<QID, 8>(dppb<QID, 4>(dppb<QID, 2>(d[0][0], d[1][1]), d[2][2]), d[3][3]);
      const float t1 = dppb<ROR4, 8>(dppb<ROR4, 4>(dppb<ROR4, 2>(dpp<ROR4>(d[0][3]), d[1][0]), d[2][1]), d[3][2]);
      const float t2 = dppb<ROR8, 8>(dppb<ROR8, 4>(dppb<ROR8, 2>(dpp<ROR8>(d[0][2]), d[1][3]), d[2][0]), d[3][1]);
      const float t3 = dppb<ROR12, 8>(dppb<ROR12, 4>(dppb<ROR12, 2>(dpp<ROR12>(d[0][1]), d[1][2]), d[2][3]), d[3][0]);
      const float s = (t0 + t1) + (t2 + t3);
      S[blk * 2 + t] = (RND_S ? rb(rb(s) * 0.08838834764831845f) : s * 0.08838834764831845f) + (blk < 5 || piece(blk, t) < 2048 ? 0.f : -__builtin_inff());
    }
    FENCE();
    ISSUE((blk + 4) * 8, (blk + 5) * 8);
    FENCE();
  }
  float mx = S[0];
#pragma unroll
  for (int u = 1; u < 18; ++u) mx = fmaxf(mx, S[u]);
  mx = fmaxf(mx, dpp<ROR4>(mx)); mx = fmaxf(mx, dpp<ROR8>(mx)); mx = rows_max(mx);
  if (lane < 4) l_mx[lane * 8 + wave] = mx;
  bar();
  ST(5);
  {
    const f32x4 m0 = *(const f32x4*)(l_mx + (lane & 3) * 8), m1 = *(const f32x4*)(l_mx + (lane & 3) * 8 + 4);
    mx = fmaxf(fmaxf(fmaxf(m0[0], m0[1]), fmaxf(m0[2], m0[3])), fmaxf(fmaxf(m1[0], m1[1]), fmaxf(m1[2], m1[3])));
  }
  constexpr float LOG2E = 1.4426950408889634f;
  unsigned P[9];
  {
    float ls = 0.f;
#pragma unroll
    for (int u = 0; u < 9; ++u) {
      const float p0 = __builtin_amdgcn_exp2f((S[2 * u] - mx) * LOG2E), p1 = __builtin_amdgcn_exp2f((S[2 * u + 1] - mx) * LOG2E);
      ls += p0 + p1;
      P[u] = pk(p0, p1);
    }
    ls += dpp<ROR4>(ls); ls += dpp<ROR8>(ls); ls = sw16(ls, ls); ls = sw32(ls, ls);
    if (lane < 4) l_ls[lane * 8 + wave] = ls;
  }
  f32x4 acc[8];
#pragma unroll
  for (int j = 0; j < 8; ++j) acc[j] = f32x4{0.f, 0.f, 0.f, 0.f};
  unsigned gap, sbp, cbp, gbp;
  const int ob = (mi >> 3) & 1, ocol = ((mi & 7) * 8 + wave) * 8 + (lane & 1) * 4 + (lane >> 5) * 2;
  auto load_mod = [&]() {
    const int off = is_mlp ? WS_MOD + (ab * 512 + kp) * 2 : OOB;
    gap = __builtin_amdgcn_raw_buffer_load_b32(rws, off, 0, SC1);
    sbp = __builtin_amdgcn_raw_buffer_load_b32(rws, off, 2048, SC1);
    cbp = __builtin_amdgcn_raw_buffer_load_b32(rws, off, 4096, SC1);
    gbp = __builtin_amdgcn_raw_buffer_load_b32(rws, is_mlp ? WS_MOD + (ob * 512 + ocol) * 2 : OOB, 6144, SC1);
  };
#pragma unroll
  for (int blk = 9; blk < 18; ++blk) {
    const unsigned p = P[blk - 9];
    const u32x4 pb = {p, dppu<SHL4>(p), dppu<SHL8>(p), dppu<SHL12>(p)};
#pragma unroll
    for (int j = 0; j < 8; ++j) {
      u32x4 va;
#pragma unroll
      for (int x = 0; x < 4; ++x) va[x] = __builtin_amdgcn_perm(win[blk & 3][4 + x][j >> 1], win[blk & 3][x][j >> 1], (j & 1) ? 0x07060302 : 0x05040100);
      acc[j] = __builtin_amdgcn_mfma_f32_16x16x32_bf16(__builtin_bit_cast(bf16x8, va), __builtin_bit_cast(bf16x8, pb), acc[j], 0, 0, 0);
    }
    FENCE();
    if (blk < 14) { ISSUE((blk + 4) * 8, (blk + 5) * 8); }
    if (blk == 14) {
      load_mod();
      if (TW == 1) load_tile(t0, is_wo ? p_wo : p_wg, is_wo ? 1024 : 3584, wave * 64 * (is_wo ? 1024 : 3584), is_wo || is_mlp);
    }
    if (blk == 14 + TW) load_tile(t1, (const char*)a.wu + (is_mlp ? mi : 0) * 128, 3584, wave * 64 * 3584, is_mlp);
    if (blk == 15 + TW) load_tile(t2, (const char*)a.wd + (is_mlp ? mi : 0) * 65536, 1024, wave * 128, is_mlp);
    FENCE();
  }
  ST(6);
  if ((lane & 12) == 0) {
#pragma unroll
    for (int j = 0; j < 8; ++j) *(f32x4*)(l_o + wave * 512 + (lane & 3) * 128 + (lane >> 4) * 32 + j * 4) = acc[j];
  }
  PASSES((DRY & 1) && wave == 0) {
  if (pass) bar();
  if (wave < 4) {
    const float* p = l_o + (lane >> 4) * 1024 + wave * 128 + (lane & 15) * 8;
    const f32x4 e = *(const f32x4*)p + *(const f32x4*)(p + 512), o = *(const f32x4*)(p + 4) + *(const f32x4*)(p + 516);
    const f32x4 l0 = *(const f32x4*)(l_ls + wave * 8), l1 = *(const f32x4*)(l_ls + wave * 8 + 4);
    const float l = ((l0[0] + l0[1]) + (l0[2] + l0[3])) + ((l1[0] + l1[1]) + (l1[2] + l1[3]));
    const float inv = rcp(l);
    const float ee = sw16(sw32(e[0], e[2]), sw32(e[1], e[3])), oo = sw16(sw32(o[0], o[2]), sw32(o[1], o[3]));
    const int base = pass ? WS_O + (g * 32 + b * 16 + split) * RS : FAR;
    __builtin_amdgcn_raw_buffer_store_b32(live(pk(ee * inv, oo * inv)), rws, base + wave * 256 + (lane & 15) * 16 + (lane >> 4) * 4, 0, SC1);
    const u32x2 ml = {live(asu(mx)), live(asu(l))};
    __builtin_amdgcn_raw_buffer_store_b64(ml, rws, lane == wave ? base + 1024 + wave * 8 : OOB, 0, SC1);
  }
  }
  ST(7);
  if (is_wo) {
    u32x4 w[8]; xpose(t0, w);
    const int src = WS_O + (g * 32 + ab * 16 + (lane >> 4)) * RS;
    const int ooff = src + wave * 128 + (((lane >> 3) & 1) * 4 + (lane & 3)) * 16, loff = src + 1024 + (wave >> 1) * 8;
    u32x4 ov[4]; u32x2 ml[4];
#pragma unroll
    for (int u = 0; u < 4; ++u) { anyval(ov[u]); anyval(ml[u]); }
    PASSES(DRY & 2) {
    if (PRE) pre_poll(rws, WS_O + (g * 32 + 16 * (wave & 1) + 15 - (wave & 6)) * RS + 1024, pass);
    bool need[4] = {true, true, true, true};
#pragma nounroll
    for (unsigned sp = pass ? 0 : SPIN_CAP; sp < SPIN_CAP; ++sp) {
      asm volatile("" ::: "memory");
#pragma unroll
      for (int u = 0; u < 4; ++u) if (need[u]) {
        ov[u] = __builtin_amdgcn_raw_buffer_load_b128(rws, ooff, u * 4 * RS, SC1);
        ml[u] = __builtin_amdgcn_raw_buffer_load_b64(rws, loff, u * 4 * RS, SC1);
      }
#pragma unroll
      for (int u = 0; u < 4; ++u) need[u] = umax3(umax3(ov[u][0], ov[u][1], ov[u][2]), umax3(ov[u][3], ml[u][0], ml[u][1]), 0) == EMPTY;
      if (__builtin_amdgcn_ballot_w64(need[0] | need[1] | need[2] | need[3]) == 0) break;
      __builtin_amdgcn_s_sleep(SLP);
    }
    ST(8);
    const float M = rows_max(fmaxf(fmaxf(asf(ml[0][0]), asf(ml[1][0])), fmaxf(asf(ml[2][0]), asf(ml[3][0]))));
    float wt[4], L = 0.f;
#pragma unroll
    for (int u = 0; u < 4; ++u) { wt[u] = __builtin_amdgcn_exp2f((asf(ml[u][0]) - M) * LOG2E) * asf(ml[u][1]); L += wt[u]; }
    L = sw16(L, L); L = sw32(L, L);
    float e[4], o[4];
#pragma unroll
    for (int r = 0; r < 4; ++r) {
      e[r] = wt[0] * blo(ov[0][r]); o[r] = wt[0] * bhi(ov[0][r]);
#pragma unroll
      for (int u = 1; u < 4; ++u) { e[r] = fmaf(wt[u], blo(ov[u][r]), e[r]); o[r] = fmaf(wt[u], bhi(ov[u][r]), o[r]); }
    }
    const float inv = rcp(L);
    put_red(l_red, wave, lane, tile_mv(w, pk(sw16(sw32(e[0], e[2]), sw32(e[1], e[3])) * inv, sw16(sw32(o[0], o[2]), sw32(o[1], o[3])) * inv)));
    asm volatile("s_waitcnt vmcnt(0)" ::: "memory");
    bar();
    if (wave == 0) {
      const f32x2 r = fin_red(l_red, lane);
      const int piece = ((fc >> 3) & 1) * 8 + (fc >> 5) * 4 + ((fc >> 1) & 3);
      const u32x2 yv = {live(asu(r[0])), live(asu(r[1]))};
      __builtin_amdgcn_raw_buffer_store_b64(yv, rws, WS_Y + (g * 8 + (i & 7)) * RS + (fb * 64 + piece * 4 + ((fc >> 4) & 1) * 2) * 4, pass ? 0 : FAR, SC1);
    }
    }
    ST(9);
  }
  if (is_mlp) {
    u32x4 wg[8], wu[8], wd[8]; xpose(t0, wg); xpose(t1, wu); xpose(t2, wd);
#pragma nounroll
    for (unsigned sp = 0; sp < SPIN_CAP; ++sp) {
      if (__builtin_amdgcn_ballot_w64((gap == EMPTY) | (sbp == EMPTY) | (cbp == EMPTY) | (gbp == EMPTY)) == 0) break;
      asm volatile("" ::: "memory");
      load_mod();
    }
    const int yoff = WS_Y + ((lane >> 5) * 32 + wave) * RS + (ab * 64 + (((lane >> 3) & 3) * 4 + (lane & 3)) * 4) * 4;
    u32x4 yv[4];
#pragma unroll
    for (int u = 0; u < 4; ++u) anyval(yv[u]);
    PASSES(DRY & 4) {
    if (PRE) pre_poll(rws, WS_Y + (g * 8 + wave) * RS, pass);
    bool need[4] = {true, true, true, true};
#pragma nounroll
    for (unsigned sp = pass ? 0 : SPIN_CAP; sp < SPIN_CAP; ++sp) {
      asm volatile("" ::: "memory");
#pragma unroll
      for (int u = 0; u < 4; ++u) if (need[u]) yv[u] = __builtin_amdgcn_raw_buffer_load_b128(rws, yoff, u * 8 * RS, SC1);
#pragma unroll
      for (int u = 0; u < 4; ++u) need[u] = umax3(umax3(yv[u][0], yv[u][1], yv[u][2]), yv[u][3], 0) == EMPTY;
      if (__builtin_amdgcn_ballot_w64(need[0] | need[1] | need[2] | need[3]) == 0) break;
      __builtin_amdgcn_s_sleep(SLP);
    }
    ST(10);
    float y[4];
#pragma unroll
    for (int r = 0; r < 4; ++r) y[r] = (asf(yv[0][r]) + asf(yv[1][r])) + (asf(yv[2][r]) + asf(yv[3][r]));
    const float xa = rb(blo(xpair) + rb(blo(gap) * rb(sw32(y[0], y[2])))), xb = rb(bhi(xpair) + rb(bhi(gap) * rb(sw32(y[1], y[3]))));
    l_x1[ab * 256 + (kp >> 1)] = pk(xa, xb);
    const float mw = sum_sameb(xa + xb) * (1.f / 64);
    const float m2 = sum_sameb((xa - mw) * (xa - mw) + (xb - mw) * (xb - mw));
    if ((lane & 59) == 0) *(f32x2*)(l_st + ab * 16 + wave * 2) = f32x2{mw, m2};
    bar();
    FST(1);
    f32x4 st[4];
#pragma unroll
    for (int u = 0; u < 4; ++u) st[u] = *(const f32x4*)(l_st + ab * 16 + u * 4);
    const float mean = (((st[0][0] + st[0][2]) + (st[1][0] + st[1][2])) + ((st[2][0] + st[2][2]) + (st[3][0] + st[3][2]))) * 0.125f;
    float dd = 0.f, mm = 0.f;
#pragma unroll
    for (int u = 0; u < 4; ++u) { const float d0 = st[u][0] - mean, d1 = st[u][2] - mean; dd += d0 * d0 + d1 * d1; mm += st[u][1] + st[u][3]; }
    const float isd = rcp(rb(__builtin_amdgcn_sqrtf(rb(rb((mm + 64.f * dd) * (1.f / 512)) + 1e-5f)))), mb = rb(mean);
    const unsigned hp = pk(modulate(xa, mb, isd, blo(cbp), blo(sbp)), modulate(xb, mb, isd, bhi(cbp), bhi(sbp)));
    FST(2);
    {
      const f32x4 rg = tile_mv(wg, hp), ru = tile_mv(wu, hp);
      float* p = l_red + wave * 256 + ((lane & 3) * 16 + ((lane >> 2) & 1) * 8 + fb * 4 + (lane >> 5) * 2) * 4;
      if (!(lane & 8)) { *(f32x4*)p = f32x4{rg[0], rg[1], ru[0], ru[1]}; *(f32x4*)(p + 4) = f32x4{rg[2], rg[3], ru[2], ru[3]}; }
    }
    bar();
    FST(3);
    f32x4 gu[8];
#pragma unroll
    for (int v = 0; v < 8; ++v) gu[v] = *(const f32x4*)(l_red + v * 256 + lane * 4);
    const f32x4 sum = ((gu[0] + gu[1]) + (gu[2] + gu[3])) + ((gu[4] + gu[5]) + (gu[6] + gu[7]));
    auto geglu = [](float gv, float uv) {
      gv = rb(gv);
      const float th = 1.f - 2.f * rcp(1.f + __expf(2.f * 0.7978845608028654f * (gv + 0.044715f * gv * gv * gv)));
      return rb(0.5f * gv * (1.f + th)) * rb(uv);
    };
    const f32x4 z = tile_mv(wd, pk(geglu(sum[0], sum[2]), geglu(sum[1], sum[3])));
    const u32x4 zo = {live(asu(z[0])), live(asu(z[1])), live(asu(z[2])), live(asu(z[3]))};
    __builtin_amdgcn_raw_buffer_store_b128(zo, rws, (lane & 8) ? OOB : WS_Z + mi * ZS + (fb * 512 + wave * 64 + (lane & 7) * 8 + (lane >> 5) * 4) * 4, pass ? 0 : FAR, SC1);
    ST(11);
    if (mi < 16) {
      const int zoff = lane < 56 ? WS_Z + (lane >> 1) * ZS + (ob * 512 + ((mi & 7) * 8 + wave) * 8 + (lane & 1) * 4) * 4 : OOB;
      const unsigned xp = l_x1[ob * 256 + (ocol >> 1)];
      u32x4 zv; anyval(zv);
#pragma nounroll
      for (unsigned sp = pass ? 0 : SPIN_CAP; sp < SPIN_CAP; ++sp) {
        asm volatile("" ::: "memory");
        zv = __builtin_amdgcn_raw_buffer_load_b128(rws, zoff, 0, SC1);
        if (!any_empty(zv)) break;
        __builtin_amdgcn_s_sleep(1);
      }
      ST(12);
      float z0 = sw32(asf(zv[0]), asf(zv[2])), z1 = sw32(asf(zv[1]), asf(zv[3]));
      z0 = sw16(z0, z0); z1 = sw16(z1, z1);
      z0 += dpp<ROR8>(z0); z1 += dpp<ROR8>(z1); z0 += dpp<ROR4>(z0); z1 += dpp<ROR4>(z1); z0 += dpp<XOR2>(z0); z1 += dpp<XOR2>(z1);
      const unsigned op = pk(blo(xp) + rb(blo(gbp) * rb(z0)), bhi(xp) + rb(bhi(gbp) * rb(z1)));
      __builtin_amdgcn_raw_buffer_store_b32(op, mk(a.out, pass ? 2048 : 0), (lane & 30) == 0 ? (ob * 512 + ocol) * 2 : OOB, 0, OAUX);
      ST(13);
    }
    }
    if (mi == 0 && tid == 0) { const u32x2 ne = {(unsigned)tag, (unsigned)(tag >> 32)}; __builtin_amdgcn_raw_buffer_store_b64(ne, rws, WS_EPOCH, 0, SC1); }
  }
}

__global__ void tiny_n() {}
extern "C" void kernel_launch(void* const* d_in, const int* in_sizes, int n_in, void* d_out, int out_size, void* d_ws, size_t ws_size, hipStream_t stream) {
  Args a;
  a.p = Ptrs{d_in[0], d_in[1], d_in[2], d_in[3], d_in[4], d_in[6], d_in[7], d_in[8], d_in[9], d_in[10], d_out, d_ws};
  for (int h = 0; h < 2; ++h)
    for (int b = 0; b < 2; ++b) {
      int def[16], n[16], sum = 0, tot = 0;
      for (int s = 0; s < 16; ++s) {
        const int i = role_of(((s >> 1) << 2) | (b << 1) | (s & 1));
        def[s] = i < 8 ? DQ : i < 16 ? DWO : i < 22 ? DMOD : (i - 22) * 8 + h * 4 < 28 ? DMLP : 0;
        sum += def[s];
      }
      for (int s = 0; s < 16; ++s) {
        n[s] = ((2048 + sum) * (1000 + ((s & 1) ? -DPAR : DPAR))) / 16000 - def[s];
        if (n[s] > 144) n[s] = 144;
        tot += n[s];
      }
      for (int s = 0; tot < 2048; s = (s + 1) & 15) if (n[s] < 144) { ++n[s]; ++tot; }
      int pool[16 * 48], np = 0, at = 0;
      for (int s = 0; s < 16; ++s) for (int p = n[s]; p < 128; ++p) pool[np++] = own_piece(((s >> 1) << 2) | (b << 1) | (s & 1), p >> 3, p & 7);
      for (int s = 0; s < 16; ++s) {
        const int extra = n[s] > 128 ? n[s] - 128 : 0, own = n[s] - extra;
        for (int w = 0; w < 8; ++w)
          a.share[(h * 32 + (((s >> 1) << 2) | (b << 1) | (s & 1))) * 8 + w] = (w < extra ? pool[at + w] : 0xFFF) | (w + 8 < extra ? pool[at + w + 8] : 0xFFF) << 12 | ((own - w + 7) >> 3) << 24;
        at += extra;
      }
    }
  tiny_n<<<8, 64, 0, stream>>>();
  fused_block<<<256, 512, 0, stream>>>(a);
}
```

```cpp
#include <hip/hip_runtime.h>
#include <hip/hip_bf16.h>
#include <stdint.h>

#define PACE 3
#define N1 4
#define N2 26
#define TW 7
#define QSKIP 1
#define ROT 1
#define DQ 25
#define DWO 12
#define DMOD 11
#define DMLP 24
#define DPAR 55
#define RMAP 1
#define SLO 0
#define SLY 0
#define SLZ 0
#define KAUX 2
#define VAUX 2
#define OAUX 0
#define SYNC 0x200
#define ZD 12
#define WAUX 2
#define RND_S 1

using u32x2 = __attribute__((ext_vector_type(2))) unsigned;
using u32x4 = __attribute__((ext_vector_type(4))) unsigned;
using f32x4 = __attribute__((ext_vector_type(4))) float;
using bf16x2 = __attribute__((ext_vector_type(2))) __bf16;
using bf16x8 = __attribute__((ext_vector_type(8))) __bf16;
using f32x2 = __attribute__((ext_vector_type(2))) float;
typedef __amdgpu_buffer_rsrc_t rsrc_t;

constexpr int SC1 = 16, NT = 2;
constexpr int WS_EPOCH = 0;
#define RS 4352
constexpr int WS_H = RS;
constexpr int WS_Q = WS_H + 32 * RS;
constexpr int WS_Y = WS_Q + 128 * RS;
constexpr int WS_MOD = WS_Y + 64 * RS;
constexpr int WS_Z = WS_MOD + 8192;
constexpr int ZS = RS > 4096 ? RS : 4096;
constexpr int WS_O = WS_Z + 32 * ZS;
constexpr int WS_BYTES = WS_O + 256 * RS;
constexpr unsigned EMPTY = 0xFFFFFFFFu;
constexpr unsigned SPIN_CAP = 1u << 18;
constexpr int OOB = 0x7FFFFFF0;

struct Ptrs {
  const void *x, *k, *v, *wq, *wo, *wg, *wu, *wd, *c, *wm;
  void *out, *ws;
};
struct Args {
  Ptrs p;
  unsigned share[512];
};

__host__ __device__ __forceinline__ int own_piece(int i, int slot, int wave) {
  const int rot = ROT == 1 ? i >> 1 : ROT == 2 ? i : ROT == 3 ? (i >> 2) : (i >> 2) * 2 + (i & 1);
  return (((i >> 2) << 1) | (i & 1)) * 128 + (((slot >> 1) + rot) & 7) * 16 + (slot & 1) * 8 + wave;
}
__host__ __device__ __forceinline__ int role_of(int pos) {
  const int j = pos >> 1;
  return !RMAP ? pos : (pos & 1) ? j + (j < 6 ? 10 : j < 10 ? 12 : 16) : j + (j < 8 ? 0 : j < 12 ? 14 : j < 14 ? 4 : -6);
}
__device__ __forceinline__ rsrc_t mk(const void* p, unsigned bytes) { return __builtin_amdgcn_make_buffer_rsrc((void*)p, 0, bytes, 0x00020000); }
__device__ __forceinline__ float asf(unsigned u) { return __builtin_bit_cast(float, u); }
__device__ __forceinline__ unsigned asu(float f) { return __builtin_bit_cast(unsigned, f); }
__device__ __forceinline__ float blo(unsigned u) { return asf(u << 16); }
__device__ __forceinline__ float bhi(unsigned u) { return asf(u & 0xffff0000u); }
__device__ __forceinline__ unsigned pk(float lo, float hi) {
  f32x2 v = {lo, hi};
  return __builtin_bit_cast(unsigned, __builtin_convertvector(v, bf16x2));
}
__device__ __forceinline__ float rb(float x) { return (float)(__bf16)x; }
__device__ __forceinline__ unsigned live(unsigned u) { return min(u, EMPTY - 1); }
__device__ __forceinline__ float dot2(unsigned a, unsigned b, float c) {
  return __builtin_amdgcn_fdot2_f32_bf16(__builtin_bit_cast(bf16x2, a), __builtin_bit_cast(bf16x2, b), c, false);
}
template <int CTRL> __device__ __forceinline__ float dpp(float x) { return asf(__builtin_amdgcn_mov_dpp(asu(x), CTRL, 0xf, 0xf, true)); }
template <int CTRL> __device__ __forceinline__ unsigned dppu(unsigned x) { return __builtin_amdgcn_mov_dpp(x, CTRL, 0xf, 0xf, true); }
template <int CTRL, int BANK> __device__ __forceinline__ float dppb(float old, float x) {
  return asf(__builtin_amdgcn_update_dpp(asu(old), asu(x), CTRL, 0xf, BANK, false));
}
constexpr int QID = 0xE4, XOR1 = 0xB1, XOR2 = 0x4E, SHL4 = 0x104, SHL8 = 0x108, SHL12 = 0x10C, SHR4 = 0x114, ROR4 = 0x124, ROR8 = 0x128, ROR12 = 0x12C;
__device__ __forceinline__ float sw32(float a, float b) {
  auto s = __builtin_amdgcn_permlane32_swap(asu(a), asu(b), false, false);
  return asf(s[0]) + asf(s[1]);
}
__device__ __forceinline__ float sw16(float a, float b) {
  auto s = __builtin_amdgcn_permlane16_swap(asu(a), asu(b), false, false);
  return asf(s[0]) + asf(s[1]);
}
__device__ __forceinline__ float rows_max(float x) {
  auto s = __builtin_amdgcn_permlane16_swap(asu(x), asu(x), false, false);
  x = fmaxf(asf(s[0]), asf(s[1]));
  auto t = __builtin_amdgcn_permlane32_swap(asu(x), asu(x), false, false);
  return fmaxf(asf(t[0]), asf(t[1]));
}
__device__ __forceinline__ float wave_sum(float x) {
  x += dpp<XOR1>(x); x += dpp<XOR2>(x); x += dpp<ROR4>(x); x += dpp<ROR8>(x);
  x = sw16(x, x);
  return sw32(x, x);
}
__device__ __forceinline__ void bar() { asm volatile("s_waitcnt lgkmcnt(0)\n\ts_barrier" ::: "memory"); }
#define FENCE() __builtin_amdgcn_sched_barrier(0)
__device__ __forceinline__ void xpose(const u32x4 (&t)[8], u32x4 (&o)[8]) {
  unsigned lo[8][2], hi[8][2];
#pragma unroll
  for (int e = 0; e < 8; ++e)
#pragma unroll
    for (int c = 0; c < 2; ++c) {
      lo[e][c] = __builtin_amdgcn_update_dpp(t[e][c], t[e][2 + c], ROR8, 0xf, 0xC, false);
      hi[e][c] = __builtin_amdgcn_update_dpp(t[e][2 + c], t[e][c], ROR8, 0xf, 0x3, false);
    }
#pragma unroll
  for (int j = 0; j < 4; ++j)
#pragma unroll
    for (int e = 0; e < 4; ++e) {
      o[j][e] = __builtin_amdgcn_perm(lo[2 * e + 1][j >> 1], lo[2 * e][j >> 1], (j & 1) ? 0x07060302 : 0x05040100);
      o[4 + j][e] = __builtin_amdgcn_perm(hi[2 * e + 1][j >> 1], hi[2 * e][j >> 1], (j & 1) ? 0x07060302 : 0x05040100);
    }
#pragma unroll
  for (int j = 0; j < 8; ++j) asm volatile("" : "+v"(o[j]));
}
struct Act { u32x4 a0, a1; };
__device__ __forceinline__ Act spread(unsigned ap) {
  const unsigned aq = dppu<ROR8>(ap);
  return Act{{dppu<0x00>(ap), dppu<0x55>(ap), dppu<0xAA>(ap), dppu<0xFF>(ap)}, {dppu<0x00>(aq), dppu<0x55>(aq), dppu<0xAA>(aq), dppu<0xFF>(aq)}};
}
__device__ __forceinline__ f32x4 tile_mv(const u32x4 (&w)[8], const Act& a) {
  f32x4 d[4];
#pragma unroll
  for (int j = 0; j < 4; ++j) d[j] = __builtin_amdgcn_mfma_f32_16x16x32_bf16(__builtin_bit_cast(bf16x8, a.a0), __builtin_bit_cast(bf16x8, w[j]), f32x4{0.f, 0.f, 0.f, 0.f}, 0, 0, 0);
#pragma unroll
  for (int j = 0; j < 4; ++j) d[j] = __builtin_amdgcn_mfma_f32_16x16x32_bf16(__builtin_bit_cast(bf16x8, a.a1), __builtin_bit_cast(bf16x8, w[4 + j]), d[j], 0, 0, 0);
  return f32x4{d[0][0], d[1][1], d[2][2], d[3][3]};
}
__device__ __forceinline__ f32x4 tile_mv(const u32x4 (&w)[8], unsigned ap) { return tile_mv(w, spread(ap)); }
__device__ __forceinline__ void put_red(float* red, int wave, int lane, const f32x4& r) {
  if (lane < 32) *(f32x4*)(red + wave * 128 + (lane >> 4) * 64 + (lane & 7) * 8 + ((lane >> 3) & 1) * 4) = r;
}
__device__ __forceinline__ void put_gu(float* red, int wave, int lane, const u32x4 (&wg)[8], const u32x4 (&wu)[8], unsigned ap) {
  const Act a = spread(ap);
  const f32x4 rg = tile_mv(wg, a), ru = tile_mv(wu, a);
  float* p = red + wave * 256 + ((lane & 3) * 16 + ((lane >> 2) & 1) * 8 + ((lane >> 4) & 1) * 4 + ((lane >> 3) & 1) * 2) * 4;
  if (lane < 32) { *(f32x4*)p = f32x4{rg[0], rg[1], ru[0], ru[1]}; *(f32x4*)(p + 4) = f32x4{rg[2], rg[3], ru[2], ru[3]}; }
}
__device__ __forceinline__ f32x4 get_gu(const float* red, int lane) {
  f32x4 v[8];
#pragma unroll
  for (int u = 0; u < 8; ++u) v[u] = *(const f32x4*)(red + u * 256 + lane * 4);
  FENCE();
  return ((v[0] + v[1]) + (v[2] + v[3])) + ((v[4] + v[5]) + (v[6] + v[7]));
}
__device__ __forceinline__ f32x2 fin_red(const float* red, int lane) {
  const float* p = red + (lane >> 5) * 512 + (lane & 31) * 4;
  const f32x4 v = (*(const f32x4*)p + *(const f32x4*)(p + 128)) + (*(const f32x4*)(p + 256) + *(const f32x4*)(p + 384));
  return f32x2{sw32(v[0], v[2]), sw32(v[1], v[3])};
}
__device__ __forceinline__ float rcp(float x) { return __builtin_amdgcn_rcpf(x); }
__device__ __forceinline__ float modulate(float x, float mean, float isd, float c, float s) { return rb(rb(rb(x - mean) * isd) * rb(1.f + c)) + s; }
__device__ __forceinline__ unsigned umax3(unsigned x, unsigned y, unsigned z) { return max(max(x, y), z); }
__device__ __forceinline__ bool any_empty(const u32x4& v) { return __builtin_amdgcn_ballot_w64(umax3(umax3(v[0], v[1], v[2]), v[3], 0) == EMPTY) != 0; }
#define POLL_LOAD(n, regs, so) "s_mov_b64 exec, " n "\n\ts_cbranch_execz 2f\n\tbuffer_load_dwordx4 " regs ", %[off], %[r], " so " offen sc1\n2:\n\t"
#define POLL_TEST(n, a, b, c, d) "v_max3_u32 %[t], " a ", " b ", " c "\n\tv_max_u32 %[t], %[t], " d "\n\tv_cmp_eq_u32 " n ", -1, %[t]\n\t"
#define POLL_END "s_cbranch_scc0 3f\n\ts_sleep %[slp]\n\ts_sub_u32 %[cnt], %[cnt], 1\n\ts_cbranch_scc0 1b\n3:"
template <int SLP> __device__ __forceinline__ void poll4(u32x4 (&v)[4], rsrc_t r, int off, int step) {
  unsigned long long n0 = ~0ull, n1 = ~0ull, n2 = ~0ull, n3 = ~0ull, any;
  unsigned cnt = SPIN_CAP * 4, t;
  asm volatile("s_nop 4\n1:\n\t"
               POLL_LOAD("%[n0]", "v[200:203]", "0") POLL_LOAD("%[n1]", "v[204:207]", "%[s1]") POLL_LOAD("%[n2]", "v[208:211]", "%[s2]") POLL_LOAD("%[n3]", "v[212:215]", "%[s3]")
               "s_mov_b64 exec, -1\n\ts_waitcnt vmcnt(0)\n\t"
               POLL_TEST("%[n0]", "v200", "v201", "v202", "v203") POLL_TEST("%[n1]", "v204", "v205", "v206", "v207")
               POLL_TEST("%[n2]", "v208", "v209", "v210", "v211") POLL_TEST("%[n3]", "v212", "v213", "v214", "v215")
               "s_or_b64 %[any], %[n0], %[n1]\n\ts_or_b64 %[any], %[any], %[n2]\n\ts_or_b64 %[any], %[any], %[n3]\n\t" POLL_END
               : "=&{v[200:203]}"(v[0]), "=&{v[204:207]}"(v[1]), "=&{v[208:211]}"(v[2]), "=&{v[212:215]}"(v[3]),
                 [n0] "+s"(n0), [n1] "+s"(n1), [n2] "+s"(n2), [n3] "+s"(n3), [any] "=&s"(any), [cnt] "+s"(cnt), [t] "=&v"(t)
               : [off] "v"(off), [r] "s"(r), [s1] "s"(step), [s2] "s"(2 * step), [s3] "s"(3 * step), [slp] "i"(SLP)
               : "memory", "scc");
}

#define ST(n)
#define FST(n)

__global__ void __launch_bounds__(512, 2) fused_block(Args args) {
  __shared__ __attribute__((aligned(16))) float lds[4096 + 2048 + 64 + 512 + 32 + 256];
  float* const l_o = lds;
  float* const l_red = lds + 4096;
  float* const l_mx = l_red + 2048;
  float* const l_ls = l_mx + 32;
  unsigned* const l_x1 = (unsigned*)(l_ls + 32);
  float* const l_st = (float*)(l_x1 + 512);
  unsigned short* const l_q = (unsigned short*)(l_st + 32);
  const int tid = threadIdx.x, lane = tid & 63, wave = __builtin_amdgcn_readfirstlane(tid >> 6);
  ST(0); FENCE();
  const int bid = blockIdx.x, g = (bid & 6) | ((bid >> 3) & 1), pos = ((bid >> 4) << 1) | (bid & 1), b = (pos >> 1) & 1, split = ((pos >> 2) << 1) | (pos & 1);
  const int i = role_of(pos);
  Ptrs a = args.p;
  unsigned share = args.share[((g >> 2) * 32 + pos) * 8 + wave];
  asm volatile("" : "+s"(a.x), "+s"(a.k), "+s"(a.v), "+s"(a.wq), "+s"(a.wo), "+s"(a.wg), "+s"(a.wu), "+s"(a.wd), "+s"(a.c), "+s"(a.wm), "+s"(a.out), "+s"(a.ws), "+s"(share));
  ST(15);
  const rsrc_t rws = mk(a.ws, WS_BYTES);
  const size_t kvbase = (size_t)(b * 8 + g) << 23;
  const rsrc_t rk = mk((const char*)a.k + kvbase, 1 << 23), rv = mk((const char*)a.v + kvbase, 1 << 23);
  const int mi = (i - 22) * 8 + g;
  const bool is_q = i < 8, is_wo = i >= 8 && i < 16, is_mod = i >= 16 && i < 22, is_mlp = i >= 22 && mi < 28;
  const int trow = ((lane >> 3) & 1) * 32 + (lane >> 4) * 8;
  const int ab = (lane >> 2) & 1, kp = wave * 64 + trow + (lane & 3) * 2;
  const int fb = (lane >> 4) & 1, fc = (lane & 15) * 4 + (lane >> 5) * 2;

  const u32x2 ep = __builtin_amdgcn_raw_buffer_load_b64(rws, WS_EPOCH, 0, SC1);
  {
    const u32x4 e4 = {EMPTY, EMPTY, EMPTY, EMPTY};
    const int r = g * 8 + (i & 7), hr = g * 2 + (i & 1), t = tid & 127;
    const int cbase = is_q ? WS_O + (r * 4 + (wave >> 1)) * RS : is_wo ? WS_Z + (r >> 1) * ZS + (r & 1) * 2048 : WS_Y + (hr * 4 + (wave >> 1)) * RS;
    const int cn = is_q ? 72 : is_wo ? (wave < 2 ? 128 : 0) : (i == 16 || i == 17) ? 32 : 0;
    __builtin_amdgcn_raw_buffer_store_b128(e4, rws, t < cn ? cbase + t * 16 : (cn == 32 && tid < 64) ? WS_MOD + hr * 512 + (tid - 32) * 16 : OOB, 0, SC1);
  }
  const rsrc_t rc = mk(a.c, 2048), rx = mk(a.x, 2048);
  const unsigned cpair = __builtin_amdgcn_raw_buffer_load_b32(rc, (ab * 512 + kp) * 2, 0, 0);
  const unsigned xpair = __builtin_amdgcn_raw_buffer_load_b32(rx, (ab * 512 + kp) * 2, 0, 0);
  const u32x4 xr0 = __builtin_amdgcn_raw_buffer_load_b128(rx, lane * 16, 0, 0), xr1 = __builtin_amdgcn_raw_buffer_load_b128(rx, lane * 16 + 1024, 0, 0);
  auto load_tile = [&](u32x4 (&t)[8], const char* p, int stride, int wave_off, bool on) {
    const rsrc_t r = mk(p, on ? 0x7FFFFFFF : 0);
    const int off = wave_off + trow * stride + (lane & 7) * 16;
#pragma unroll
    for (int e = 0; e < 8; ++e) t[e] = __builtin_amdgcn_raw_buffer_load_b128(r, off, e * stride, WAUX);
  };
  const char* const p_wo = (const char*)a.wo + (g * 512 * 512 + (i & 7) * 64) * 2;
  const char* const p_wg = (const char*)a.wg + (is_mlp ? mi : 0) * 128;
  u32x4 t0[8], t1[8], t2[8];
  {
    const char* p = is_q ? (const char*)a.wq + (g * 512 + i * 64) * 2 : is_mod ? (const char*)a.wm + ((i - 16) * 512 + g * 64) * 2 : is_wo ? p_wo : p_wg;
    const int stride = is_q ? 8192 : is_mod ? 6144 : is_wo ? 1024 : 3584;
    load_tile(t0, p, stride, wave * 64 * stride, is_q || is_mod);
  }

  u32x4 win[4][8];
  const int koff = ((lane >> 2) & 3) * 256 + ((lane >> 4) * 4 + (lane & 3)) * 16, voff = lane * 16;
  auto piece = [&](int blk, int t) {
    const int u = blk % 9, own = own_piece(pos, u * 2 + t, wave);
    return u == 8 ? (int)(share >> (12 * t)) & 0xFFF : u < 5 || u * 2 + t < (int)(share >> 24) ? own : 0xFFF;
  };
  auto issue1 = [&](int blk, int n) {
    const int so = piece(blk, n >> 2) * 4096;
    if (blk < 9) win[blk & 3][n] = __builtin_amdgcn_raw_buffer_load_b128(rk, koff + (n & 3) * 1024, so, KAUX);
    else win[blk & 3][n] = __builtin_amdgcn_raw_buffer_load_b128(rv, voff + (n & 3) * 1024, so, VAUX);
  };
#define ISSUE(from, to) _Pragma("unroll") for (int p_ = (from); p_ < (to); ++p_) issue1(p_ >> 3, p_ & 7)
#define MISSUE(from, to) ISSUE(from, to); if ((SYNC >> ((from) / 8 - 4)) & 1) { FENCE(); asm volatile("s_barrier" ::: "memory"); }
#define PACED(from, to, skip) _Pragma("unroll") for (int p_ = (from); p_ < (to); ++p_) { \
    FENCE(); asm volatile("s_cmp_lt_u32 %1, %2\n\ts_cbranch_scc1 1f\n\ts_waitcnt vmcnt(%0)\n1:" :: "i"(PACE - 1), "s"(i), "i"(skip) : "memory", "scc"); issue1(p_ >> 3, p_ & 7); FENCE(); }
  FENCE();
  ISSUE(0, PACE);
  FENCE();

  const unsigned long long tag = ((((unsigned long long)__builtin_amdgcn_readfirstlane(ep[1]) << 32) | (unsigned)__builtin_amdgcn_readfirstlane(ep[0])) + 1);
  const unsigned tag_lo = (unsigned)tag << 16, tag_hi = (unsigned)(tag >> 16);
  auto stale = [&](unsigned lo, unsigned hi) { return (hi != tag_hi) | ((lo & 0xffff0000u) != tag_lo); };
  unsigned modv = 0;
  if (is_mod) {
    auto silu = [](float v) { return v * rcp(1.f + __expf(-v)); };
    u32x4 w[8]; xpose(t0, w);
    put_red(l_red, wave, lane, tile_mv(w, pk(silu(blo(cpair)), silu(bhi(cpair)))));
    asm volatile("s_waitcnt vmcnt(0)" ::: "memory");
    bar();
    if (wave == 0) {
      const f32x2 r = fin_red(l_red, lane);
      modv = pk(r[0], r[1]);
      const u32x4 gr = {tag_lo | (modv & 0xffffu), tag_hi, tag_lo | (modv >> 16), tag_hi};
      __builtin_amdgcn_raw_buffer_store_b128(gr, rws, i < 18 ? WS_H + (((i - 16) * 2 + fb) * 8 + g) * RS + fc * 8 : OOB, 0, SC1);
    }
    ST(1);
  }
  PACED(PACE, N1, 0);
  if (is_q) {
    u32x4 w[8]; xpose(t0, w);
    float s0 = 0.f, s1 = 0.f;
#pragma unroll
    for (int e = 0; e < 4; ++e) { s0 += blo(xr0[e]) + bhi(xr0[e]); s1 += blo(xr1[e]) + bhi(xr1[e]); }
    const float m0 = wave_sum(s0) * (1.f / 512), m1 = wave_sum(s1) * (1.f / 512);
    float q0 = 0.f, q1 = 0.f;
#pragma unroll
    for (int e = 0; e < 4; ++e) {
      float d = blo(xr0[e]) - m0; q0 += d * d; d = bhi(xr0[e]) - m0; q0 += d * d;
      d = blo(xr1[e]) - m1; q1 += d * d; d = bhi(xr1[e]) - m1; q1 += d * d;
    }
    const float v0 = wave_sum(q0) * (1.f / 512), v1 = wave_sum(q1) * (1.f / 512);
    const float mean = rb(ab ? m1 : m0), isd = rcp(rb(__builtin_amdgcn_sqrtf(rb(rb(ab ? v1 : v0) + 1e-5f))));
    const int hoff = WS_H + (ab * 8 + wave) * RS + (kp & 63) * 8;
    u32x4 sa, ca;
    unsigned sp = 0;
#pragma nounroll
    for (;;) {
      asm volatile("" ::: "memory");
      sa = __builtin_amdgcn_raw_buffer_load_b128(rws, hoff, 0, SC1); ca = __builtin_amdgcn_raw_buffer_load_b128(rws, hoff, 16 * RS, SC1);
      if (__builtin_amdgcn_ballot_w64(stale(sa[0], sa[1]) | stale(sa[2], sa[3]) | stale(ca[0], ca[1]) | stale(ca[2], ca[3])) == 0 || ++sp >= SPIN_CAP) break;
      __builtin_amdgcn_s_sleep(2);
    }
    ST(2);
    put_red(l_red, wave, lane, tile_mv(w, pk(modulate(blo(xpair), mean, isd, blo(ca[0]), blo(sa[0])), modulate(bhi(xpair), mean, isd, blo(ca[2]), blo(sa[2])))));
    asm volatile("s_waitcnt vmcnt(0)" ::: "memory");
    bar();
    if (wave == 0) {
      const f32x2 r = fin_red(l_red, lane);
      const unsigned qp = pk(r[0], r[1]);
      const u32x4 gr = {tag_lo | (qp & 0xffffu), tag_hi, tag_lo | (qp >> 16), tag_hi};
      __builtin_amdgcn_raw_buffer_store_b128(gr, rws, WS_Q + ((g * 2 + fb) * 8 + i) * RS + fc * 8, 0, SC1);
    }
    ST(3);
  }
  PACED(N1, N2, QSKIP * 8);
  {
    const int off = WS_Q + ((g * 2 + b) * 8 + wave) * RS + lane * 8;
    u32x2 qg = __builtin_amdgcn_raw_buffer_load_b64(rws, off, 0, SC1);
    FENCE();
    ISSUE(N2, 32);
    FENCE();
    if (__builtin_amdgcn_ballot_w64(stale(qg[0], qg[1])) != 0) {
      unsigned sp = 0;
#pragma nounroll
      do {
        __builtin_amdgcn_s_sleep(2);
        asm volatile("" ::: "memory");
        qg = __builtin_amdgcn_raw_buffer_load_b64(rws, off, 0, SC1);
      } while (__builtin_amdgcn_ballot_w64(stale(qg[0], qg[1])) != 0 && ++sp < SPIN_CAP);
    }
    l_q[tid] = (unsigned short)qg[0];
  }
  bar();
  const u32x4 qf = *(const u32x4*)(l_q + (lane & 3) * 128 + ((lane >> 4) * 4 + ((lane >> 2) & 3)) * 8);
  __builtin_amdgcn_raw_buffer_store_b32(live(modv), rws, (is_mod && i >= 18 && wave == 0) ? WS_MOD + (((i - 18) * 2 + fb) * 512 + g * 64 + fc) * 2 : OOB, 0, SC1);
  ST(4);

  float S[18];
#pragma unroll
  for (int blk = 0; blk < 9; ++blk) {
#pragma unroll
    for (int t = 0; t < 2; ++t) {
      f32x4 d[4];
#pragma unroll
      for (int x = 0; x < 4; ++x)
        d[x] = __builtin_amdgcn_mfma_f32_16x16x32_bf16(__builtin_bit_cast(bf16x8, win[blk & 3][t * 4 + x]), __builtin_bit_cast(bf16x8, qf), f32x4{0.f, 0.f, 0.f, 0.f}, 0, 0, 0);
      const float t0 = dppb<QID, 8>(dppb<QID, 4>(dppb<QID, 2>(d[0][0], d[1][1]), d[2][2]), d[3][3]);
      const float t1 = dppb<ROR4, 8>(dppb<ROR4, 4>(dppb<ROR4, 2>(dpp<ROR4>(d[0][3]), d[1][0]), d[2][1]), d[3][2]);
      const float t2 = dppb<ROR8, 8>(dppb<ROR8, 4>(dppb<ROR8, 2>(dpp<ROR8>(d[0][2]), d[1][3]), d[2][0]), d[3][1]);
      const float t3 = dppb<ROR12, 8>(dppb<ROR12, 4>(dppb<ROR12, 2>(dpp<ROR12>(d[0][1]), d[1][2]), d[2][3]), d[3][0]);
      const float s = (t0 + t1) + (t2 + t3);
      S[blk * 2 + t] = (RND_S ? rb(rb(s) * 0.08838834764831845f) : s * 0.08838834764831845f) + (blk < 5 || piece(blk, t) < 2048 ? 0.f : -__builtin_inff());
    }
    FENCE();
    MISSUE((blk + 4) * 8, (blk + 5) * 8);
    FENCE();
  }
  float mx = S[0];
#pragma unroll
  for (int u = 1; u < 18; ++u) mx = fmaxf(mx, S[u]);
  mx = fmaxf(mx, dpp<ROR4>(mx)); mx = fmaxf(mx, dpp<ROR8>(mx)); mx = rows_max(mx);
  if (lane < 4) l_mx[lane * 8 + wave] = mx;
  bar();
  ST(5);
  {
    const f32x4 m0 = *(const f32x4*)(l_mx + (lane & 3) * 8), m1 = *(const f32x4*)(l_mx + (lane & 3) * 8 + 4);
    mx = fmaxf(fmaxf(fmaxf(m0[0], m0[1]), fmaxf(m0[2], m0[3])), fmaxf(fmaxf(m1[0], m1[1]), fmaxf(m1[2], m1[3])));
  }
  constexpr float LOG2E = 1.4426950408889634f;
  const int obase = WS_O + (g * 32 + b * 16 + split) * RS;
  unsigned P[9];
  {
    float ls = 0.f;
#pragma unroll
    for (int u = 0; u < 9; ++u) {
      const float p0 = __builtin_amdgcn_exp2f((S[2 * u] - mx) * LOG2E), p1 = __builtin_amdgcn_exp2f((S[2 * u + 1] - mx) * LOG2E);
      ls += p0 + p1;
      P[u] = pk(p0, p1);
    }
    ls += dpp<ROR4>(ls); ls += dpp<ROR8>(ls); ls = sw16(ls, ls); ls = sw32(ls, ls);
    if (lane < 4) l_ls[lane * 8 + wave] = ls;
    bar();
    const f32x4 l4 = *(const f32x4*)(l_ls + (lane & 3) * 8) + *(const f32x4*)(l_ls + (lane & 3) * 8 + 4);
    const u32x2 ml = {live(asu(mx)), live(asu((l4[0] + l4[1]) + (l4[2] + l4[3])))};
    __builtin_amdgcn_raw_buffer_store_b64(ml, rws, tid < 4 ? obase + 1024 + tid * 8 : OOB, 0, SC1);
  }
  f32x4 acc[8];
#pragma unroll
  for (int j = 0; j < 8; ++j) acc[j] = f32x4{0.f, 0.f, 0.f, 0.f};
  unsigned gap, sbp, cbp, gbp;
  const int ob = (mi >> 3) & 1, ocol = (mi & 7) * 64 + (wave & 1) * 32 + (lane & 7) * 4 + (lane >> 5) * 2;
  auto load_mod = [&]() {
    const int off = is_mlp ? WS_MOD + (ab * 512 + kp) * 2 : OOB;
    gap = __builtin_amdgcn_raw_buffer_load_b32(rws, off, 0, SC1);
    sbp = __builtin_amdgcn_raw_buffer_load_b32(rws, off, 2048, SC1);
    cbp = __builtin_amdgcn_raw_buffer_load_b32(rws, off, 4096, SC1);
    gbp = __builtin_amdgcn_raw_buffer_load_b32(rws, is_mlp ? WS_MOD + (ob * 512 + ocol) * 2 : OOB, 6144, SC1);
  };
#pragma unroll
  for (int blk = 9; blk < 18; ++blk) {
    const unsigned p = P[blk - 9];
    const u32x4 pb = {p, dppu<SHL4>(p), dppu<SHL8>(p), dppu<SHL12>(p)};
#pragma unroll
    for (int j = 0; j < 8; ++j) {
      u32x4 va;
#pragma unroll
      for (int x = 0; x < 4; ++x) va[x] = __builtin_amdgcn_perm(win[blk & 3][4 + x][j >> 1], win[blk & 3][x][j >> 1], (j & 1) ? 0x07060302 : 0x05040100);
      acc[j] = __builtin_amdgcn_mfma_f32_16x16x32_bf16(__builtin_bit_cast(bf16x8, va), __builtin_bit_cast(bf16x8, pb), acc[j], 0, 0, 0);
    }
    FENCE();
    if (blk < 14) { MISSUE((blk + 4) * 8, (blk + 5) * 8); }
    if (blk == 14) {
      load_mod();
      if (TW & 3) load_tile(t0, is_wo ? p_wo : p_wg, is_wo ? 1024 : 3584, wave * 64 * (is_wo ? 1024 : 3584), ((TW & 1) && is_wo) || ((TW & 2) && is_mlp));
    }
    if (blk == 15 && (TW & 4)) load_tile(t1, (const char*)a.wu + (is_mlp ? mi : 0) * 128, 3584, wave * 64 * 3584, is_mlp);
    if (blk == 16 && (TW & 8)) load_tile(t2, (const char*)a.wd + (is_mlp ? mi : 0) * 65536, 1024, wave * 128, is_mlp);
    FENCE();
  }
  ST(6);
  if ((lane & 12) == 0) {
#pragma unroll
    for (int j = 0; j < 8; ++j) *(f32x4*)(l_o + wave * 512 + (lane & 3) * 128 + (lane >> 4) * 32 + j * 4) = acc[j];
  }
  bar();
  if (wave < 4) {
    const float* p = l_o + (lane >> 4) * 1024 + wave * 128 + (lane & 15) * 8;
    const f32x4 e = *(const f32x4*)p + *(const f32x4*)(p + 512), o = *(const f32x4*)(p + 4) + *(const f32x4*)(p + 516);
    const float ee = sw16(sw32(e[0], e[2]), sw32(e[1], e[3])), oo = sw16(sw32(o[0], o[2]), sw32(o[1], o[3]));
    __builtin_amdgcn_raw_buffer_store_b32(live(pk(ee, oo)), rws, obase + wave * 256 + (lane & 15) * 16 + (lane >> 4) * 4, 0, SC1);
  }
  ST(7);
  if (is_wo) {
    if (!(TW & 1)) { bar(); load_tile(t0, p_wo, 1024, wave * 65536, true); }
    const int src = WS_O + (g * 32 + ab * 16 + (lane >> 4)) * RS;
    const int ooff = src + wave * 128 + (((lane >> 3) & 1) * 4 + (lane & 3)) * 16, loff = src + 1024 + (wave >> 1) * 8;
    u32x2 ml[4];
#pragma nounroll
    for (unsigned sp = 0; sp < SPIN_CAP; ++sp) {
      asm volatile("" ::: "memory");
#pragma unroll
      for (int u = 0; u < 4; ++u) ml[u] = __builtin_amdgcn_raw_buffer_load_b64(rws, loff, u * 4 * RS, SC1);
      if (!any_empty(u32x4{max(ml[0][0], ml[0][1]), max(ml[1][0], ml[1][1]), max(ml[2][0], ml[2][1]), max(ml[3][0], ml[3][1])})) break;
      __builtin_amdgcn_s_sleep(2);
    }
    const float M = rows_max(fmaxf(fmaxf(asf(ml[0][0]), asf(ml[1][0])), fmaxf(asf(ml[2][0]), asf(ml[3][0]))));
    f32x4 wt;
    float L = 0.f;
#pragma unroll
    for (int u = 0; u < 4; ++u) { wt[u] = __builtin_amdgcn_exp2f((asf(ml[u][0]) - M) * LOG2E); L = fmaf(wt[u], asf(ml[u][1]), L); }
    L = sw16(L, L); L = sw32(L, L);
    wt *= rcp(L);
    asm volatile("" : "+v"(wt));
    u32x4 w[8]; xpose(t0, w);
    u32x4 ov[4];
    poll4<SLO>(ov, rws, ooff, 4 * RS);
    ST(8);
    f32x2 eo[4];
#pragma unroll
    for (int r = 0; r < 4; ++r) {
      eo[r] = wt[0] * f32x2{blo(ov[0][r]), bhi(ov[0][r])};
#pragma unroll
      for (int u = 1; u < 4; ++u) eo[r] += wt[u] * f32x2{blo(ov[u][r]), bhi(ov[u][r])};
    }
    put_red(l_red, wave, lane, tile_mv(w, pk(sw16(sw32(eo[0][0], eo[2][0]), sw32(eo[1][0], eo[3][0])), sw16(sw32(eo[0][1], eo[2][1]), sw32(eo[1][1], eo[3][1])))));
    asm volatile("s_waitcnt vmcnt(0)" ::: "memory");
    bar();
    if (wave == 0) {
      const f32x2 r = fin_red(l_red, lane);
      const int piece = ((fc >> 3) & 1) * 8 + (fc >> 5) * 4 + ((fc >> 1) & 3);
      const u32x2 yv = {live(asu(r[0])), live(asu(r[1]))};
      __builtin_amdgcn_raw_buffer_store_b64(yv, rws, WS_Y + (g * 8 + (i & 7)) * RS + (fb * 64 + piece * 4 + ((fc >> 4) & 1) * 2) * 4, 0, SC1);
    }
    ST(9);
  }
  if (is_mlp) {
    bar();
    if (!(TW & 2)) load_tile(t0, p_wg, 3584, wave * 64 * 3584, true);
    if (!(TW & 4)) load_tile(t1, (const char*)a.wu + mi * 128, 3584, wave * 64 * 3584, true);
    if (!(TW & 8)) load_tile(t2, (const char*)a.wd + mi * 65536, 1024, wave * 128, true);
    u32x4 wg[8], wu[8], wd[8]; xpose(t0, wg); xpose(t1, wu);
#pragma nounroll
    for (unsigned sp = 0; sp < SPIN_CAP; ++sp) {
      if (__builtin_amdgcn_ballot_w64((gap == EMPTY) | (sbp == EMPTY) | (cbp == EMPTY) | (gbp == EMPTY)) == 0) break;
      asm volatile("" ::: "memory");
      load_mod();
    }
    const unsigned c1 = pk(1.f + blo(cbp), 1.f + bhi(cbp));
    put_gu(l_o, wave, lane, wg, wu, c1);
    put_gu(l_o + 2048, wave, lane, wg, wu, sbp);
    bar();
    f32x4 gc = get_gu(l_o, lane), gs = get_gu(l_o + 2048, lane);
    f32x2 k0 = {blo(xpair) * blo(c1), bhi(xpair) * bhi(c1)}, k1 = {blo(gap) * blo(c1), bhi(gap) * bhi(c1)}, x0 = {blo(xpair), bhi(xpair)}, ga = {blo(gap), bhi(gap)};
    asm volatile("" : "+v"(gc), "+v"(gs), "+v"(k0), "+v"(k1), "+v"(x0), "+v"(ga));
    xpose(t2, wd);
    const int yoff = WS_Y + ((lane >> 5) * 32 + wave) * RS + (ab * 64 + (((lane >> 3) & 3) * 4 + (lane & 3)) * 4) * 4;
    u32x4 yv[4];
    poll4<SLY>(yv, rws, yoff, 8 * RS);
    ST(10);
    auto f4 = [](const u32x4& v) { return __builtin_bit_cast(f32x4, v); };
    const f32x4 y4 = (f4(yv[0]) + f4(yv[1])) + (f4(yv[2]) + f4(yv[3]));
    const f32x2 y = {sw32(y4[0], y4[2]), sw32(y4[1], y4[3])};
    const f32x2 x1 = ga * y + x0, ac = k1 * y + k0;
    float s1 = x1[0] + x1[1], s2 = x1[0] * x1[0] + x1[1] * x1[1];
    s1 += dpp<XOR1>(s1); s2 += dpp<XOR1>(s2); s1 += dpp<XOR2>(s1); s2 += dpp<XOR2>(s2); s1 += dpp<ROR8>(s1); s2 += dpp<ROR8>(s2);
    s1 = sw16(s1, s2); s1 = sw32(s1, s1);
    put_gu(l_red, wave, lane, wg, wu, pk(ac[0], ac[1]));
    l_st[ab * 16 + wave * 2 + fb] = s1;
    l_x1[ab * 256 + (kp >> 1)] = pk(x1[0], x1[1]);
    bar();
    FST(14);
    const f32x4* sp = (const f32x4*)(l_st + ab * 16);
    const f32x4 st0 = sp[0], st1 = sp[1], st2 = sp[2], st3 = sp[3], g1 = get_gu(l_red, lane);
    const f32x4 st = (st0 + st1) + (st2 + st3);
    const float mean = (st[0] + st[2]) * (1.f / 512), var = (st[1] + st[3]) * (1.f / 512) - mean * mean;
    const f32x4 gu = (g1 - mean * gc) * __builtin_amdgcn_rsqf(fmaxf(var, 0.f) + 1e-5f) + gs;
    auto geglu = [](float gv, float uv) {
      constexpr float C = -2.f * 0.7978845608028654f * LOG2E;
      return gv * uv * rcp(1.f + __builtin_amdgcn_exp2f(gv * fmaf(gv * gv, 0.044715f * C, C)));
    };
    const f32x4 z = tile_mv(wd, pk(geglu(gu[0], gu[2]), geglu(gu[1], gu[3])));
    const u32x4 zo = {live(asu(z[0])), live(asu(z[1])), live(asu(z[2])), live(asu(z[3]))};
    __builtin_amdgcn_raw_buffer_store_b128(zo, rws, lane >= 32 ? OOB : WS_Z + mi * ZS + (fb * 512 + wave * 64 + (lane & 7) * 8 + ((lane >> 3) & 1) * 4) * 4, 0, SC1);
    ST(11);
    if (mi < 16 && wave < 2) {
      const unsigned xp = l_x1[ob * 256 + (ocol >> 1)];
      u32x4 zv[4];
      if (ZD) __builtin_amdgcn_s_sleep(ZD);
      poll4<SLZ>(zv, mk(a.ws, WS_Z + 28 * ZS), WS_Z + (lane >> 3) * ZS + (ob * 512 + (mi & 7) * 64 + wave * 32 + (lane & 7) * 4) * 4, 8 * ZS);
      ST(12);
      const f32x4 z4 = (f4(zv[0]) + f4(zv[1])) + (f4(zv[2]) + f4(zv[3]));
      float z0 = sw32(z4[0], z4[2]), z1 = sw32(z4[1], z4[3]);
      z0 += dpp<ROR8>(z0); z1 += dpp<ROR8>(z1);
      z0 = sw16(z0, z0); z1 = sw16(z1, z1);
      const unsigned op = pk(fmaf(blo(gbp), z0, blo(xp)), fmaf(bhi(gbp), z1, bhi(xp)));
      __builtin_amdgcn_raw_buffer_store_b32(op, mk(a.out, 2048), (lane & 24) == 0 ? (ob * 512 + ocol) * 2 : OOB, 0, OAUX);
      ST(13);
    }
    if (mi == 0 && tid == 0) { const u32x2 ne = {(unsigned)tag, (unsigned)(tag >> 32)}; __builtin_amdgcn_raw_buffer_store_b64(ne, rws, WS_EPOCH, 0, SC1); }
  }
}

extern "C" void kernel_launch(void* const* d_in, const int* in_sizes, int n_in, void* d_out, int out_size, void* d_ws, size_t ws_size, hipStream_t stream) {
  Args a;
  a.p = Ptrs{d_in[0], d_in[1], d_in[2], d_in[3], d_in[4], d_in[6], d_in[7], d_in[8], d_in[9], d_in[10], d_out, d_ws};
  for (int h = 0; h < 2; ++h)
    for (int b = 0; b < 2; ++b) {
      int def[16], n[16], sum = 0, tot = 0;
      for (int s = 0; s < 16; ++s) {
        const int i = role_of(((s >> 1) << 2) | (b << 1) | (s & 1));
        def[s] = i < 8 ? DQ : i < 16 ? DWO : i < 22 ? DMOD : (i - 22) * 8 + h * 4 < 28 ? DMLP : 0;
        sum += def[s];
      }
      for (int s = 0; s < 16; ++s) {
        n[s] = ((2048 + sum) * (1000 + ((s & 1) ? -DPAR : DPAR))) / 16000 - def[s];
        if (n[s] > 144) n[s] = 144;
        tot += n[s];
      }
      for (int s = 0; tot < 2048; s = (s + 1) & 15) if (n[s] < 144) { ++n[s]; ++tot; }
      int pool[16 * 48], np = 0, at = 0;
      for (int s = 0; s < 16; ++s) for (int p = n[s]; p < 128; ++p) pool[np++] = own_piece(((s >> 1) << 2) | (b << 1) | (s & 1), p >> 3, p & 7);
      for (int s = 0; s < 16; ++s) {
        const int extra = n[s] > 128 ? n[s] - 128 : 0, own = n[s] - extra;
        for (int w = 0; w < 8; ++w)
          a.share[(h * 32 + (((s >> 1) << 2) | (b << 1) | (s & 1))) * 8 + w] = (w < extra ? pool[at + w] : 0xFFF) | (w + 8 < extra ? pool[at + w + 8] : 0xFFF) << 12 | ((own - w + 7) >> 3) << 24;
        at += extra;
      }
    }
  fused_block<<<256, 512, 0, stream>>>(a);
}
```

```cpp
#include <hip/hip_runtime.h>
#include <hip/hip_bf16.h>
#include <stdint.h>

constexpr int PACE = 3;
constexpr int N1 = 4, N2 = 26;
constexpr int DQ = 29, DWO = 7, DMOD = 14, DMLP = 24, DS = 6, DPAR = 50;
constexpr int THR = 15, TQ = 3030, TWE = 2760, TWO = 2810, TME = 2950, TMO = 2985, TMLP = 2660, TNO = 2905, TSL = 40, TSM = 80;
constexpr int OH = 4100, YH = 4230;


using u32x2 = __attribute__((ext_vector_type(2))) unsigned;
using u32x4 = __attribute__((ext_vector_type(4))) unsigned;
using f32x4 = __attribute__((ext_vector_type(4))) float;
using bf16x2 = __attribute__((ext_vector_type(2))) __bf16;
using bf16x8 = __attribute__((ext_vector_type(8))) __bf16;
using f32x2 = __attribute__((ext_vector_type(2))) float;
typedef __amdgpu_buffer_rsrc_t rsrc_t;

constexpr int SC1 = 16, NT = 2;
constexpr int WS_EPOCH = 0;
constexpr int RS = 5120;
constexpr int WS_H = RS + 512;
constexpr int WS_Q = WS_H + 32 * RS;
constexpr int WS_Y = WS_Q + 128 * RS;
constexpr int WS_MOD = WS_Y + 64 * RS;
constexpr int WS_Z = WS_MOD + 8192;
constexpr int ZS = RS;
constexpr int WS_O = WS_Z + 32 * ZS;
constexpr int WS_BYTES = WS_O + 256 * RS;
constexpr unsigned EMPTY = 0xFFFFFFFFu;
constexpr unsigned SPIN_CAP = 1u << 18;
constexpr int OOB = 0x7FFFFFF0;

struct Ptrs {
  const void *x, *k, *v, *wq, *wo, *wg, *wu, *wd, *c, *wm;
  void *out, *ws;
};
struct Args {
  Ptrs p;
  unsigned share[512];
};

__host__ __device__ __forceinline__ int own_piece(int i, int slot, int wave) {
  return (((i >> 2) << 1) | (i & 1)) * 128 + (((slot >> 1) + (i >> 1)) & 7) * 16 + (slot & 1) * 8 + wave;
}
__host__ __device__ __forceinline__ int role_of(int pos) {
  const int j = pos >> 1;
  return (pos & 1) ? j + (j < 6 ? 10 : j < 10 ? 12 : 16) : j + (j < 8 ? 0 : j < 12 ? 14 : j < 14 ? 4 : -6);
}
__device__ __forceinline__ rsrc_t mk(const void* p, unsigned bytes) { return __builtin_amdgcn_make_buffer_rsrc((void*)p, 0, bytes, 0x00020000); }
__device__ __forceinline__ float asf(unsigned u) { return __builtin_bit_cast(float, u); }
__device__ __forceinline__ unsigned asu(float f) { return __builtin_bit_cast(unsigned, f); }
__device__ __forceinline__ float blo(unsigned u) { return asf(u << 16); }
__device__ __forceinline__ float bhi(unsigned u) { return asf(u & 0xffff0000u); }
__device__ __forceinline__ unsigned pk(float lo, float hi) {
  f32x2 v = {lo, hi};
  return __builtin_bit_cast(unsigned, __builtin_convertvector(v, bf16x2));
}
__device__ __forceinline__ float rb(float x) { return (float)(__bf16)x; }
__device__ __forceinline__ unsigned live(unsigned u) { return min(u, EMPTY - 1); }
__device__ __forceinline__ float dot2(unsigned a, unsigned b, float c) {
  return __builtin_amdgcn_fdot2_f32_bf16(__builtin_bit_cast(bf16x2, a), __builtin_bit_cast(bf16x2, b), c, false);
}
template <int CTRL> __device__ __forceinline__ float dpp(float x) { return asf(__builtin_amdgcn_mov_dpp(asu(x), CTRL, 0xf, 0xf, true)); }
template <int CTRL> __device__ __forceinline__ unsigned dppu(unsigned x) { return __builtin_amdgcn_mov_dpp(x, CTRL, 0xf, 0xf, true); }
template <int CTRL, int BANK> __device__ __forceinline__ float dppb(float old, float x) {
  return asf(__builtin_amdgcn_update_dpp(asu(old), asu(x), CTRL, 0xf, BANK, false));
}
constexpr int QID = 0xE4, XOR1 = 0xB1, XOR2 = 0x4E, SHL4 = 0x104, SHL8 = 0x108, SHL12 = 0x10C, SHR4 = 0x114, ROR4 = 0x124, ROR8 = 0x128, ROR12 = 0x12C;
__device__ __forceinline__ float sw32(float a, float b) {
  auto s = __builtin_amdgcn_permlane32_swap(asu(a), asu(b), false, false);
  return asf(s[0]) + asf(s[1]);
}
__device__ __forceinline__ float sw16(float a, float b) {
  auto s = __builtin_amdgcn_permlane16_swap(asu(a), asu(b), false, false);
  return asf(s[0]) + asf(s[1]);
}
__device__ __forceinline__ float rows_max(float x) {
  auto s = __builtin_amdgcn_permlane16_swap(asu(x), asu(x), false, false);
  x = fmaxf(asf(s[0]), asf(s[1]));
  auto t = __builtin_amdgcn_permlane32_swap(asu(x), asu(x), false, false);
  return fmaxf(asf(t[0]), asf(t[1]));
}
__device__ __forceinline__ float wave_sum(float x) {
  x += dpp<XOR1>(x); x += dpp<XOR2>(x); x += dpp<ROR4>(x); x += dpp<ROR8>(x);
  x = sw16(x, x);
  return sw32(x, x);
}
__device__ __forceinline__ void bar() { asm volatile("s_waitcnt lgkmcnt(0)\n\ts_barrier" ::: "memory"); }
#define FENCE() __builtin_amdgcn_sched_barrier(0)
__device__ __forceinline__ void xpose(const u32x4 (&t)[8], u32x4 (&o)[8]) {
  unsigned lo[8][2], hi[8][2];
#pragma unroll
  for (int e = 0; e < 8; ++e)
#pragma unroll
    for (int c = 0; c < 2; ++c) {
      lo[e][c] = __builtin_amdgcn_update_dpp(t[e][c], t[e][2 + c], ROR8, 0xf, 0xC, false);
      hi[e][c] = __builtin_amdgcn_update_dpp(t[e][2 + c], t[e][c], ROR8, 0xf, 0x3, false);
    }
#pragma unroll
  for (int j = 0; j < 4; ++j)
#pragma unroll
    for (int e = 0; e < 4; ++e) {
      o[j][e] = __builtin_amdgcn_perm(lo[2 * e + 1][j >> 1], lo[2 * e][j >> 1], (j & 1) ? 0x07060302 : 0x05040100);
      o[4 + j][e] = __builtin_amdgcn_perm(hi[2 * e + 1][j >> 1], hi[2 * e][j >> 1], (j & 1) ? 0x07060302 : 0x05040100);
    }
#pragma unroll
  for (int j = 0; j < 8; ++j) asm volatile("" : "+v"(o[j]));
}
struct Act { u32x4 a0, a1; };
__device__ __forceinline__ Act spread(unsigned ap) {
  const unsigned aq = dppu<ROR8>(ap);
  return Act{{dppu<0x00>(ap), dppu<0x55>(ap), dppu<0xAA>(ap), dppu<0xFF>(ap)}, {dppu<0x00>(aq), dppu<0x55>(aq), dppu<0xAA>(aq), dppu<0xFF>(aq)}};
}
__device__ __forceinline__ f32x4 tile_mv(const u32x4 (&w)[8], const Act& a) {
  f32x4 d[4];
#pragma unroll
  for (int j = 0; j < 4; ++j) d[j] = __builtin_amdgcn_mfma_f32_16x16x32_bf16(__builtin_bit_cast(bf16x8, a.a0), __builtin_bit_cast(bf16x8, w[j]), f32x4{0.f, 0.f, 0.f, 0.f}, 0, 0, 0);
#pragma unroll
  for (int j = 0; j < 4; ++j) d[j] = __builtin_amdgcn_mfma_f32_16x16x32_bf16(__builtin_bit_cast(bf16x8, a.a1), __builtin_bit_cast(bf16x8, w[4 + j]), d[j], 0, 0, 0);
  return f32x4{d[0][0], d[1][1], d[2][2], d[3][3]};
}
__device__ __forceinline__ f32x4 tile_mv(const u32x4 (&w)[8], unsigned ap) { return tile_mv(w, spread(ap)); }
__device__ __forceinline__ void put_red(float* red, int wave, int lane, const f32x4& r) {
  if (lane < 32) *(f32x4*)(red + wave * 128 + (lane >> 4) * 64 + (lane & 7) * 8 + ((lane >> 3) & 1) * 4) = r;
}
__device__ __forceinline__ void put_gu(float* red, int wave, int lane, const u32x4 (&wg)[8], const u32x4 (&wu)[8], unsigned ap) {
  const Act a = spread(ap);
  const f32x4 rg = tile_mv(wg, a), ru = tile_mv(wu, a);
  float* p = red + wave * 256 + ((lane & 3) * 16 + ((lane >> 2) & 1) * 8 + ((lane >> 4) & 1) * 4 + ((lane >> 3) & 1) * 2) * 4;
  if (lane < 32) { *(f32x4*)p = f32x4{rg[0], rg[1], ru[0], ru[1]}; *(f32x4*)(p + 4) = f32x4{rg[2], rg[3], ru[2], ru[3]}; }
}
__device__ __forceinline__ f32x4 get_gu(const float* red, int lane) {
  f32x4 v[8];
#pragma unroll
  for (int u = 0; u < 8; ++u) v[u] = *(const f32x4*)(red + u * 256 + lane * 4);
  FENCE();
  return ((v[0] + v[1]) + (v[2] + v[3])) + ((v[4] + v[5]) + (v[6] + v[7]));
}
__device__ __forceinline__ f32x2 fin_red(const float* red, int lane) {
  const float* p = red + (lane >> 5) * 512 + (lane & 31) * 4;
  const f32x4 v = (*(const f32x4*)p + *(const f32x4*)(p + 128)) + (*(const f32x4*)(p + 256) + *(const f32x4*)(p + 384));
  return f32x2{sw32(v[0], v[2]), sw32(v[1], v[3])};
}
__device__ __forceinline__ float rcp(float x) { return __builtin_amdgcn_rcpf(x); }
__device__ __forceinline__ float modulate(float x, float mean, float isd, float c, float s) { return rb(rb(rb(x - mean) * isd) * rb(1.f + c)) + s; }
__device__ __forceinline__ unsigned umax3(unsigned x, unsigned y, unsigned z) { return max(max(x, y), z); }
__device__ __forceinline__ bool any_empty(const u32x4& v) { return __builtin_amdgcn_ballot_w64(umax3(umax3(v[0], v[1], v[2]), v[3], 0) == EMPTY) != 0; }
#define POLL_LOAD(n, regs, so) "s_mov_b64 exec, " n "\n\ts_cbranch_execz 2f\n\tbuffer_load_dwordx4 " regs ", %[off], %[r], " so " offen sc1\n2:\n\t"
#define POLL_TEST(n, a, b, c, d) "v_max3_u32 %[t], " a ", " b ", " c "\n\tv_max_u32 %[t], %[t], " d "\n\tv_cmp_eq_u32 " n ", -1, %[t]\n\t"
#define POLL_END "s_cbranch_scc0 3f\n\ts_sub_u32 %[cnt], %[cnt], 1\n\ts_cbranch_scc0 1b\n3:"
__device__ __forceinline__ void poll4(u32x4 (&v)[4], rsrc_t r, int off, int step) {
  unsigned long long n0 = ~0ull, n1 = ~0ull, n2 = ~0ull, n3 = ~0ull, any;
  unsigned cnt = SPIN_CAP * 4, t;
  asm volatile("s_nop 4\n1:\n\t"
               POLL_LOAD("%[n0]", "v[200:203]", "0") POLL_LOAD("%[n1]", "v[204:207]", "%[s1]") POLL_LOAD("%[n2]", "v[208:211]", "%[s2]") POLL_LOAD("%[n3]", "v[212:215]", "%[s3]")
               "s_mov_b64 exec, -1\n\ts_waitcnt vmcnt(0)\n\t"
               POLL_TEST("%[n0]", "v200", "v201", "v202", "v203") POLL_TEST("%[n1]", "v204", "v205", "v206", "v207")
               POLL_TEST("%[n2]", "v208", "v209", "v210", "v211") POLL_TEST("%[n3]", "v212", "v213", "v214", "v215")
               "s_or_b64 %[any], %[n0], %[n1]\n\ts_or_b64 %[any], %[any], %[n2]\n\ts_or_b64 %[any], %[any], %[n3]\n\t" POLL_END
               : "=&{v[200:203]}"(v[0]), "=&{v[204:207]}"(v[1]), "=&{v[208:211]}"(v[2]), "=&{v[212:215]}"(v[3]),
                 [n0] "+s"(n0), [n1] "+s"(n1), [n2] "+s"(n2), [n3] "+s"(n3), [any] "=&s"(any), [cnt] "+s"(cnt), [t] "=&v"(t)
               : [off] "v"(off), [r] "s"(r), [s1] "s"(step), [s2] "s"(2 * step), [s3] "s"(3 * step)
               : "memory", "scc");
}

__global__ void __launch_bounds__(512, 2) fused_block(Args args) {
  __shared__ __attribute__((aligned(16))) float lds[4096 + 2048 + 64 + 512 + 32 + 256];
  float* const l_o = lds;
  float* const l_red = lds + 4096;
  float* const l_mx = l_red + 2048;
  float* const l_ls = l_mx + 32;
  unsigned* const l_x1 = (unsigned*)(l_ls + 32);
  float* const l_st = (float*)(l_x1 + 512);
  unsigned short* const l_q = (unsigned short*)(l_st + 32);
  const int tid = threadIdx.x, lane = tid & 63, wave = __builtin_amdgcn_readfirstlane(tid >> 6);
  const unsigned born = (unsigned)__builtin_amdgcn_s_memrealtime();
  auto hold = [&](unsigned due) { while ((unsigned)__builtin_amdgcn_s_memrealtime() - born < due) __builtin_amdgcn_s_sleep(2); };
  const int bid = blockIdx.x, g = (bid & 6) | ((bid >> 3) & 1), pos = ((bid >> 4) << 1) | (bid & 1), b = (pos >> 1) & 1, split = ((pos >> 2) << 1) | (pos & 1);
  const int i = role_of(pos);
  Ptrs a = args.p;
  unsigned share = args.share[((g == 7) * 32 + pos) * 8 + wave];
  asm volatile("" : "+s"(a.x), "+s"(a.k), "+s"(a.v), "+s"(a.wq), "+s"(a.wo), "+s"(a.wg), "+s"(a.wu), "+s"(a.wd), "+s"(a.c), "+s"(a.wm), "+s"(a.out), "+s"(a.ws), "+s"(share));
  const rsrc_t rws = mk(a.ws, WS_BYTES);
  const size_t kvbase = (size_t)(b * 8 + g) << 23;
  const rsrc_t rk = mk((const char*)a.k + kvbase, 1 << 23), rv = mk((const char*)a.v + kvbase, 1 << 23);
  const int mi = g * 4 + ((i - 22) ^ 1), mc = g ^ 4;
  const bool is_q = i < 8, is_wo = i >= 8 && i < 16, is_mod = i >= 16 && i < 22, is_mlp = i >= 22 && i < 26 && g < 7;
  const int trow = ((lane >> 3) & 1) * 32 + (lane >> 4) * 8;
  const int ab = (lane >> 2) & 1, kp = wave * 64 + trow + (lane & 3) * 2;
  const int fb = (lane >> 4) & 1, fc = (lane & 15) * 4 + (lane >> 5) * 2;

  const u32x2 ep = __builtin_amdgcn_raw_buffer_load_b64(rws, WS_EPOCH, 0, SC1);
  {
    const u32x4 e4 = {EMPTY, EMPTY, EMPTY, EMPTY};
    const int r = g * 8 + (i & 7), hr = g * 2 + (i & 1), t = tid & 127;
    const int cbase = is_q ? WS_O + (r * 4 + (wave >> 1)) * RS : is_wo ? WS_Z + (r >> 1) * ZS + (r & 1) * 2048 : WS_Y + (hr * 4 + (wave >> 1)) * RS;
    const int cn = is_q ? 72 : is_wo ? (wave < 2 ? 128 : 0) : (i == 16 || i == 17) ? 32 : 0;
    __builtin_amdgcn_raw_buffer_store_b128(e4, rws, t < cn ? cbase + t * 16 : (cn == 32 && tid < 64) ? WS_MOD + hr * 512 + (tid - 32) * 16 : OOB, 0, SC1);
  }
  const rsrc_t rc = mk(a.c, 2048), rx = mk(a.x, 2048);
  const unsigned cpair = __builtin_amdgcn_raw_buffer_load_b32(rc, (ab * 512 + kp) * 2, 0, 0);
  const unsigned xpair = __builtin_amdgcn_raw_buffer_load_b32(rx, (ab * 512 + kp) * 2, 0, 0);
  const u32x4 xr0 = __builtin_amdgcn_raw_buffer_load_b128(rx, lane * 16, 0, 0), xr1 = __builtin_amdgcn_raw_buffer_load_b128(rx, lane * 16 + 1024, 0, 0);
  auto load_tile = [&](u32x4 (&t)[8], const char* p, int stride, int wave_off, bool on) {
    const rsrc_t r = mk(p, on ? 0x7FFFFFFF : 0);
    const int off = wave_off + trow * stride + (lane & 7) * 16;
#pragma unroll
    for (int e = 0; e < 8; ++e) t[e] = __builtin_amdgcn_raw_buffer_load_b128(r, off, e * stride, NT);
  };
  const char* const p_wo = (const char*)a.wo + (g * 512 * 512 + (i & 7) * 64) * 2;
  const char* const p_wg = (const char*)a.wg + (is_mlp ? mi : 0) * 128;
  u32x4 t0[8], t1[8], t2[8];
  {
    const char* p = is_q ? (const char*)a.wq + (g * 512 + i * 64) * 2 : is_mod ? (const char*)a.wm + ((i - 16) * 512 + mc * 64) * 2 : is_wo ? p_wo : p_wg;
    const int stride = is_q ? 8192 : is_mod ? 6144 : is_wo ? 1024 : 3584;
    load_tile(t0, p, stride, wave * 64 * stride, is_q || is_mod);
  }

  u32x4 win[4][8];
  const int koff = ((lane >> 2) & 3) * 256 + ((lane >> 4) * 4 + (lane & 3)) * 16, voff = lane * 16;
  auto piece = [&](int blk, int t) {
    const int u = blk % 9, own = own_piece(pos, u * 2 + t, wave);
    return u == 8 ? (int)(share >> (12 * t)) & 0xFFF : u < 5 || u * 2 + t < (int)(share >> 24) ? own : 0xFFF;
  };
  auto issue1 = [&](int blk, int n) {
    const int so = (piece(blk, n >> 2) ^ g) * 4096, io = (n & 3) * 1024;
    if (blk < 9) win[blk & 3][n] = __builtin_amdgcn_raw_buffer_load_b128(rk, koff + io, so, NT);
    else win[blk & 3][n] = __builtin_amdgcn_raw_buffer_load_b128(rv, voff + io, so, NT);
  };
#define ISSUE(from, to) _Pragma("unroll") for (int p_ = (from); p_ < (to); ++p_) issue1(p_ >> 3, p_ & 7)
#define PACED(from, to, skip) _Pragma("unroll") for (int p_ = (from); p_ < (to); ++p_) { \
    FENCE(); asm volatile("s_cmp_lt_u32 %1, %2\n\ts_cbranch_scc1 1f\n\ts_waitcnt vmcnt(%0)\n1:" :: "i"(PACE - 1), "s"(i), "i"(skip) : "memory", "scc"); issue1(p_ >> 3, p_ & 7); FENCE(); }
  FENCE();
  ISSUE(0, PACE);
  FENCE();

  const unsigned long long tag = ((((unsigned long long)__builtin_amdgcn_readfirstlane(ep[1]) << 32) | (unsigned)__builtin_amdgcn_readfirstlane(ep[0])) + 1);
  const unsigned tag_lo = (unsigned)tag << 16, tag_hi = (unsigned)(tag >> 16);
  auto stale = [&](unsigned lo, unsigned hi) { return (hi != tag_hi) | ((lo & 0xffff0000u) != tag_lo); };
  unsigned modv = 0;
  if (is_mod) {
    auto silu = [](float v) { return v * rcp(1.f + __expf(-v)); };
    u32x4 w[8]; xpose(t0, w);
    put_red(l_red, wave, lane, tile_mv(w, pk(silu(blo(cpair)), silu(bhi(cpair)))));
    asm volatile("s_waitcnt vmcnt(0)" ::: "memory");
    bar();
    if (wave == 0) {
      const f32x2 r = fin_red(l_red, lane);
      modv = pk(r[0], r[1]);
      const u32x4 gr = {tag_lo | (modv & 0xffffu), tag_hi, tag_lo | (modv >> 16), tag_hi};
      __builtin_amdgcn_raw_buffer_store_b128(gr, rws, i < 18 ? WS_H + (((i - 16) * 2 + fb) * 8 + mc) * RS + fc * 8 : OOB, 0, SC1);
    }
  }
  PACED(PACE, N1, 0);
  if (is_q) {
    u32x4 w[8]; xpose(t0, w);
    float s0 = 0.f, s1 = 0.f;
#pragma unroll
    for (int e = 0; e < 4; ++e) { s0 += blo(xr0[e]) + bhi(xr0[e]); s1 += blo(xr1[e]) + bhi(xr1[e]); }
    const float m0 = wave_sum(s0) * (1.f / 512), m1 = wave_sum(s1) * (1.f / 512);
    float q0 = 0.f, q1 = 0.f;
#pragma unroll
    for (int e = 0; e < 4; ++e) {
      float d = blo(xr0[e]) - m0; q0 += d * d; d = bhi(xr0[e]) - m0; q0 += d * d;
      d = blo(xr1[e]) - m1; q1 += d * d; d = bhi(xr1[e]) - m1; q1 += d * d;
    }
    const float v0 = wave_sum(q0) * (1.f / 512), v1 = wave_sum(q1) * (1.f / 512);
    const float mean = rb(ab ? m1 : m0), isd = rcp(rb(__builtin_amdgcn_sqrtf(rb(rb(ab ? v1 : v0) + 1e-5f))));
    const int hoff = WS_H + (ab * 8 + wave) * RS + (kp & 63) * 8;
    u32x4 sa, ca;
    unsigned sp = 0;
#pragma nounroll
    for (;;) {
      asm volatile("" ::: "memory");
      sa = __builtin_amdgcn_raw_buffer_load_b128(rws, hoff, 0, SC1); ca = __builtin_amdgcn_raw_buffer_load_b128(rws, hoff, 16 * RS, SC1);
      if (__builtin_amdgcn_ballot_w64(stale(sa[0], sa[1]) | stale(sa[2], sa[3]) | stale(ca[0], ca[1]) | stale(ca[2], ca[3])) == 0 || ++sp >= SPIN_CAP) break;
      __builtin_amdgcn_s_sleep(2);
    }
    put_red(l_red, wave, lane, tile_mv(w, pk(modulate(blo(xpair), mean, isd, blo(ca[0]), blo(sa[0])), modulate(bhi(xpair), mean, isd, blo(ca[2]), blo(sa[2])))));
    asm volatile("s_waitcnt vmcnt(0)" ::: "memory");
    bar();
    if (wave == 0) {
      const f32x2 r = fin_red(l_red, lane);
      const unsigned qp = pk(r[0], r[1]);
      const u32x4 gr = {tag_lo | (qp & 0xffffu), tag_hi, tag_lo | (qp >> 16), tag_hi};
      __builtin_amdgcn_raw_buffer_store_b128(gr, rws, WS_Q + ((g * 2 + fb) * 8 + i) * RS + fc * 8, 0, SC1);
    }
  }
  PACED(N1, N2, 8);
  {
    const int off = WS_Q + ((g * 2 + b) * 8 + wave) * RS + lane * 8;
    u32x2 qg = __builtin_amdgcn_raw_buffer_load_b64(rws, off, 0, SC1);
    FENCE();
    ISSUE(N2, 32);
    FENCE();
    if (__builtin_amdgcn_ballot_w64(stale(qg[0], qg[1])) != 0) {
      unsigned sp = 0;
#pragma nounroll
      do {
        __builtin_amdgcn_s_sleep(2);
        asm volatile("" ::: "memory");
        qg = __builtin_amdgcn_raw_buffer_load_b64(rws, off, 0, SC1);
      } while (__builtin_amdgcn_ballot_w64(stale(qg[0], qg[1])) != 0 && ++sp < SPIN_CAP);
    }
    l_q[tid] = (unsigned short)qg[0];
  }
  bar();
  const u32x4 qf = *(const u32x4*)(l_q + (lane & 3) * 128 + ((lane >> 4) * 4 + ((lane >> 2) & 3)) * 8);
  __builtin_amdgcn_raw_buffer_store_b32(live(modv), rws, (is_mod && i >= 18 && wave == 0) ? WS_MOD + (((i - 18) * 2 + fb) * 512 + mc * 64 + fc) * 2 : OOB, 0, SC1);

  float S[18];
#pragma unroll
  for (int blk = 0; blk < 9; ++blk) {
#pragma unroll
    for (int t = 0; t < 2; ++t) {
      f32x4 d[4];
#pragma unroll
      for (int x = 0; x < 4; ++x)
        d[x] = __builtin_amdgcn_mfma_f32_16x16x32_bf16(__builtin_bit_cast(bf16x8, win[blk & 3][t * 4 + x]), __builtin_bit_cast(bf16x8, qf), f32x4{0.f, 0.f, 0.f, 0.f}, 0, 0, 0);
      const float t0 = dppb<QID, 8>(dppb<QID, 4>(dppb<QID, 2>(d[0][0], d[1][1]), d[2][2]), d[3][3]);
      const float t1 = dppb<ROR4, 8>(dppb<ROR4, 4>(dppb<ROR4, 2>(dpp<ROR4>(d[0][3]), d[1][0]), d[2][1]), d[3][2]);
      const float t2 = dppb<ROR8, 8>(dppb<ROR8, 4>(dppb<ROR8, 2>(dpp<ROR8>(d[0][2]), d[1][3]), d[2][0]), d[3][1]);
      const float t3 = dppb<ROR12, 8>(dppb<ROR12, 4>(dppb<ROR12, 2>(dpp<ROR12>(d[0][1]), d[1][2]), d[2][3]), d[3][0]);
      const float s = (t0 + t1) + (t2 + t3);
      S[blk * 2 + t] = rb(rb(s) * 0.08838834764831845f) + (blk < 5 || piece(blk, t) < 2048 ? 0.f : -__builtin_inff());
    }
    FENCE();
    ISSUE((blk + 4) * 8, (blk + 5) * 8);
    FENCE();
  }
  float mx = S[0];
#pragma unroll
  for (int u = 1; u < 18; ++u) mx = fmaxf(mx, S[u]);
  mx = fmaxf(mx, dpp<ROR4>(mx)); mx = fmaxf(mx, dpp<ROR8>(mx)); mx = rows_max(mx);
  if (lane < 4) l_mx[lane * 8 + wave] = mx;
  if (wave == 0) hold(THR + (i < 8 ? TQ : i < 10 ? TWE : i < 16 ? TWO : i < 18 ? TME : i < 22 ? TMO : is_mlp ? TMLP : TNO) - (i == 11 ? TSL : i == 24 ? TSM : 0));
  bar();
  {
    const f32x4 m0 = *(const f32x4*)(l_mx + (lane & 3) * 8), m1 = *(const f32x4*)(l_mx + (lane & 3) * 8 + 4);
    mx = fmaxf(fmaxf(fmaxf(m0[0], m0[1]), fmaxf(m0[2], m0[3])), fmaxf(fmaxf(m1[0], m1[1]), fmaxf(m1[2], m1[3])));
  }
  constexpr float LOG2E = 1.4426950408889634f;
  const int obase = WS_O + (g * 32 + b * 16 + split) * RS;
  unsigned P[9];
  {
    float ls = 0.f;
#pragma unroll
    for (int u = 0; u < 9; ++u) {
      const float p0 = __builtin_amdgcn_exp2f((S[2 * u] - mx) * LOG2E), p1 = __builtin_amdgcn_exp2f((S[2 * u + 1] - mx) * LOG2E);
      ls += p0 + p1;
      P[u] = pk(p0, p1);
    }
    ls += dpp<ROR4>(ls); ls += dpp<ROR8>(ls); ls = sw16(ls, ls); ls = sw32(ls, ls);
    if (lane < 4) l_ls[lane * 8 + wave] = ls;
    bar();
    const f32x4 l4 = *(const f32x4*)(l_ls + (lane & 3) * 8) + *(const f32x4*)(l_ls + (lane & 3) * 8 + 4);
    const u32x2 ml = {live(asu(mx)), live(asu((l4[0] + l4[1]) + (l4[2] + l4[3])))};
    __builtin_amdgcn_raw_buffer_store_b64(ml, rws, tid < 4 ? obase + 1024 + tid * 8 : OOB, 0, SC1);
  }
  f32x4 acc[8];
#pragma unroll
  for (int j = 0; j < 8; ++j) acc[j] = f32x4{0.f, 0.f, 0.f, 0.f};
  unsigned gap, sbp, cbp, gbp;
  const int ob = (mi >> 3) & 1, ocol = (mi & 7) * 64 + (wave & 1) * 32 + (lane & 7) * 4 + (lane >> 5) * 2;
  auto load_mod = [&]() {
    const int off = is_mlp ? WS_MOD + (ab * 512 + kp) * 2 : OOB;
    gap = __builtin_amdgcn_raw_buffer_load_b32(rws, off, 0, SC1);
    sbp = __builtin_amdgcn_raw_buffer_load_b32(rws, off, 2048, SC1);
    cbp = __builtin_amdgcn_raw_buffer_load_b32(rws, off, 4096, SC1);
    gbp = __builtin_amdgcn_raw_buffer_load_b32(rws, is_mlp ? WS_MOD + (ob * 512 + ocol) * 2 : OOB, 6144, SC1);
  };
  const int src = WS_O + (g * 32 + ab * 16 + (lane >> 4)) * RS;
  u32x2 ml[4];
  auto load_ml = [&]() {
#pragma unroll
    for (int u = 0; u < 4; ++u) ml[u] = __builtin_amdgcn_raw_buffer_load_b64(rws, is_wo ? src + 1024 + (wave >> 1) * 8 : OOB, u * 4 * RS, SC1);
  };
#pragma unroll
  for (int blk = 9; blk < 18; ++blk) {
    const unsigned p = P[blk - 9];
    const u32x4 pb = {p, dppu<SHL4>(p), dppu<SHL8>(p), dppu<SHL12>(p)};
#pragma unroll
    for (int j = 0; j < 8; ++j) {
      u32x4 va;
#pragma unroll
      for (int x = 0; x < 4; ++x) va[x] = __builtin_amdgcn_perm(win[blk & 3][4 + x][j >> 1], win[blk & 3][x][j >> 1], (j & 1) ? 0x07060302 : 0x05040100);
      acc[j] = __builtin_amdgcn_mfma_f32_16x16x32_bf16(__builtin_bit_cast(bf16x8, va), __builtin_bit_cast(bf16x8, pb), acc[j], 0, 0, 0);
    }
    FENCE();
    if (blk < 14) { ISSUE((blk + 4) * 8, (blk + 5) * 8); }
    if (blk == 9) { FENCE(); asm volatile("s_barrier" ::: "memory"); }
    if (blk == 14) {
      load_mod();
      load_tile(t0, is_wo ? p_wo : p_wg, is_wo ? 1024 : 3584, wave * 64 * (is_wo ? 1024 : 3584), is_wo || is_mlp);
    }
    if (blk == 15) load_tile(t1, (const char*)a.wu + (is_mlp ? mi : 0) * 128, 3584, wave * 64 * 3584, is_mlp);
    if (blk == 17) load_ml();
    FENCE();
  }
  if ((lane & 12) == 0) {
#pragma unroll
    for (int j = 0; j < 8; ++j) *(f32x4*)(l_o + wave * 512 + (lane & 3) * 128 + (lane >> 4) * 32 + j * 4) = acc[j];
  }
  bar();
  if (wave < 4) {
    const float* p = l_o + (lane >> 4) * 1024 + wave * 128 + (lane & 15) * 8;
    const f32x4 e = *(const f32x4*)p + *(const f32x4*)(p + 512), o = *(const f32x4*)(p + 4) + *(const f32x4*)(p + 516);
    const float ee = sw16(sw32(e[0], e[2]), sw32(e[1], e[3])), oo = sw16(sw32(o[0], o[2]), sw32(o[1], o[3]));
    __builtin_amdgcn_raw_buffer_store_b32(live(pk(ee, oo)), rws, obase + wave * 256 + (lane & 15) * 16 + (lane >> 4) * 4, 0, SC1);
  }
  if (is_wo) {
    const int ooff = src + wave * 128 + (((lane >> 3) & 1) * 4 + (lane & 3)) * 16;
#pragma nounroll
    for (unsigned sp = 0; sp < SPIN_CAP; ++sp) {
      if (!any_empty(u32x4{max(ml[0][0], ml[0][1]), max(ml[1][0], ml[1][1]), max(ml[2][0], ml[2][1]), max(ml[3][0], ml[3][1])})) break;
      asm volatile("" ::: "memory");
      load_ml();
    }
    const float M = rows_max(fmaxf(fmaxf(asf(ml[0][0]), asf(ml[1][0])), fmaxf(asf(ml[2][0]), asf(ml[3][0]))));
    f32x4 wt;
    float L = 0.f;
#pragma unroll
    for (int u = 0; u < 4; ++u) { wt[u] = __builtin_amdgcn_exp2f((asf(ml[u][0]) - M) * LOG2E); L = fmaf(wt[u], asf(ml[u][1]), L); }
    L = sw16(L, L); L = sw32(L, L);
    wt *= rcp(L);
    asm volatile("" : "+v"(wt));
    u32x4 w[8]; xpose(t0, w);
    u32x4 ov[4];
    hold(OH);
    poll4(ov, rws, ooff, 4 * RS);
    f32x2 eo[4];
#pragma unroll
    for (int r = 0; r < 4; ++r) {
      eo[r] = wt[0] * f32x2{blo(ov[0][r]), bhi(ov[0][r])};
#pragma unroll
      for (int u = 1; u < 4; ++u) eo[r] += wt[u] * f32x2{blo(ov[u][r]), bhi(ov[u][r])};
    }
    put_red(l_red, wave, lane, tile_mv(w, pk(sw16(sw32(eo[0][0], eo[2][0]), sw32(eo[1][0], eo[3][0])), sw16(sw32(eo[0][1], eo[2][1]), sw32(eo[1][1], eo[3][1])))));
    asm volatile("s_waitcnt vmcnt(0)" ::: "memory");
    bar();
    if (wave == 0) {
      const f32x2 r = fin_red(l_red, lane);
      const int piece = ((fc >> 3) & 1) * 8 + (fc >> 5) * 4 + ((fc >> 1) & 3);
      const u32x2 yv = {live(asu(r[0])), live(asu(r[1]))};
      __builtin_amdgcn_raw_buffer_store_b64(yv, rws, WS_Y + (g * 8 + (i & 7)) * RS + (fb * 64 + piece * 4 + ((fc >> 4) & 1) * 2) * 4, 0, SC1);
    }
  }
  if (is_mlp) {
    bar();
    load_tile(t2, (const char*)a.wd + mi * 65536, 1024, wave * 128, true);
    u32x4 wg[8], wu[8], wd[8]; xpose(t0, wg); xpose(t1, wu);
#pragma nounroll
    for (unsigned sp = 0; sp < SPIN_CAP; ++sp) {
      if (__builtin_amdgcn_ballot_w64((gap == EMPTY) | (sbp == EMPTY) | (cbp == EMPTY) | (gbp == EMPTY)) == 0) break;
      asm volatile("" ::: "memory");
      load_mod();
    }
    const unsigned c1 = pk(1.f + blo(cbp), 1.f + bhi(cbp));
    put_gu(l_o, wave, lane, wg, wu, c1);
    put_gu(l_o + 2048, wave, lane, wg, wu, sbp);
    bar();
    f32x4 gc = get_gu(l_o, lane), gs = get_gu(l_o + 2048, lane);
    f32x2 k0 = {blo(xpair) * blo(c1), bhi(xpair) * bhi(c1)}, k1 = {blo(gap) * blo(c1), bhi(gap) * bhi(c1)}, x0 = {blo(xpair), bhi(xpair)}, ga = {blo(gap), bhi(gap)};
    asm volatile("" : "+v"(gc), "+v"(gs), "+v"(k0), "+v"(k1), "+v"(x0), "+v"(ga));
    xpose(t2, wd);
    const int yoff = WS_Y + ((lane >> 5) * 32 + wave) * RS + (ab * 64 + (((lane >> 3) & 3) * 4 + (lane & 3)) * 4) * 4;
    u32x4 yv[4];
    hold(YH);
    poll4(yv, rws, yoff, 8 * RS);
    auto f4 = [](const u32x4& v) { return __builtin_bit_cast(f32x4, v); };
    const f32x4 y4 = (f4(yv[0]) + f4(yv[1])) + (f4(yv[2]) + f4(yv[3]));
    const f32x2 y = {sw32(y4[0], y4[2]), sw32(y4[1], y4[3])};
    const f32x2 x1 = ga * y + x0, ac = k1 * y + k0;
    float s1 = x1[0] + x1[1], s2 = x1[0] * x1[0] + x1[1] * x1[1];
    s1 += dpp<XOR1>(s1); s2 += dpp<XOR1>(s2); s1 += dpp<XOR2>(s1); s2 += dpp<XOR2>(s2); s1 += dpp<ROR8>(s1); s2 += dpp<ROR8>(s2);
    s1 = sw16(s1, s2); s1 = sw32(s1, s1);
    put_gu(l_red, wave, lane, wg, wu, pk(ac[0], ac[1]));
    l_st[ab * 16 + wave * 2 + fb] = s1;
    l_x1[ab * 256 + (kp >> 1)] = pk(x1[0], x1[1]);
    bar();
    const f32x4* sp = (const f32x4*)(l_st + ab * 16);
    const f32x4 st0 = sp[0], st1 = sp[1], st2 = sp[2], st3 = sp[3], g1 = get_gu(l_red, lane);
    const f32x4 st = (st0 + st1) + (st2 + st3);
    const float mean = (st[0] + st[2]) * (1.f / 512), var = (st[1] + st[3]) * (1.f / 512) - mean * mean;
    const f32x4 gu = (g1 - mean * gc) * __builtin_amdgcn_rsqf(fmaxf(var, 0.f) + 1e-5f) + gs;
    auto geglu = [](float gv, float uv) {
      constexpr float C = -2.f * 0.7978845608028654f * LOG2E;
      return gv * uv * rcp(1.f + __builtin_amdgcn_exp2f(gv * fmaf(gv * gv, 0.044715f * C, C)));
    };
    const f32x4 z = tile_mv(wd, pk(geglu(gu[0], gu[2]), geglu(gu[1], gu[3])));
    const u32x4 zo = {live(asu(z[0])), live(asu(z[1])), live(asu(z[2])), live(asu(z[3]))};
    __builtin_amdgcn_raw_buffer_store_b128(zo, rws, lane >= 32 ? OOB : WS_Z + mi * ZS + (fb * 512 + wave * 64 + (lane & 7) * 8 + ((lane >> 3) & 1) * 4) * 4, 0, SC1);
    if (mi < 16 && wave < 2) {
      const unsigned xp = l_x1[ob * 256 + (ocol >> 1)];
      u32x4 zv[4];
      __builtin_amdgcn_s_sleep(12);
      poll4(zv, mk(a.ws, WS_Z + 28 * ZS), WS_Z + (lane >> 3) * ZS + (ob * 512 + (mi & 7) * 64 + wave * 32 + (lane & 7) * 4) * 4, 8 * ZS);
      const f32x4 z4 = (f4(zv[0]) + f4(zv[1])) + (f4(zv[2]) + f4(zv[3]));
      float z0 = sw32(z4[0], z4[2]), z1 = sw32(z4[1], z4[3]);
      z0 += dpp<ROR8>(z0); z1 += dpp<ROR8>(z1);
      z0 = sw16(z0, z0); z1 = sw16(z1, z1);
      const unsigned op = pk(fmaf(blo(gbp), z0, blo(xp)), fmaf(bhi(gbp), z1, bhi(xp)));
      __builtin_amdgcn_raw_buffer_store_b32(op, mk(a.out, 2048), (lane & 24) == 0 ? (ob * 512 + ocol) * 2 : OOB, 0, 0);
    }
    if (mi == 20 && tid == 0) { const u32x2 ne = {(unsigned)tag, (unsigned)(tag >> 32)}; __builtin_amdgcn_raw_buffer_store_b64(ne, rws, WS_EPOCH, 0, SC1); }
  }
}

extern "C" void kernel_launch(void* const* d_in, const int* in_sizes, int n_in, void* d_out, int out_size, void* d_ws, size_t ws_size, hipStream_t stream) {
  Args a;
  a.p = Ptrs{d_in[0], d_in[1], d_in[2], d_in[3], d_in[4], d_in[6], d_in[7], d_in[8], d_in[9], d_in[10], d_out, d_ws};
  for (int h = 0; h < 2; ++h)
    for (int b = 0; b < 2; ++b) {
      int def[16], n[16], sum = 0, tot = 0;
      for (int s = 0; s < 16; ++s) {
        const int i = role_of(((s >> 1) << 2) | (b << 1) | (s & 1));
        def[s] = i < 8 ? DQ : i < 16 ? DWO + (i == 11) * DS : i < 22 ? DMOD + h * DS : i < 26 && !h ? DMLP + (i == 24) * DS : 0;
        sum += def[s];
      }
      for (int s = 0; s < 16; ++s) {
        n[s] = ((2048 + sum) * (1000 + ((s & 1) ? -DPAR : DPAR))) / 16000 - def[s];
        if (n[s] > 144) n[s] = 144;
        tot += n[s];
      }
      for (int s = 0; tot < 2048; s = (s + 1) & 15) if (n[s] < 144) { ++n[s]; ++tot; }
      int pool[16 * 48], np = 0, at = 0;
      for (int s = 0; s < 16; ++s) for (int p = n[s]; p < 128; ++p) pool[np++] = own_piece(((s >> 1) << 2) | (b << 1) | (s & 1), p >> 3, p & 7);
      for (int s = 0; s < 16; ++s) {
        const int extra = n[s] > 128 ? n[s] - 128 : 0, own = n[s] - extra;
        for (int w = 0; w < 8; ++w)
          a.share[(h * 32 + (((s >> 1) << 2) | (b << 1) | (s & 1))) * 8 + w] = (w < extra ? pool[at + w] : 0xFFF) | (w + 8 < extra ? pool[at + w + 8] : 0xFFF) << 12 | ((own - w + 7) >> 3) << 24;
        at += extra;
      }
    }
  fused_block<<<256, 512, 0, stream>>>(a);
}
```
